```python
import torch

def _softmax(x, axis=-1):
    return torch.softmax(x, dim=axis)
def _gelu(x):
    return torch.nn.functional.gelu(x, approximate="tanh")
def _silu(x):
    return torch.nn.functional.silu(x)
def _sigmoid(x):
    return torch.sigmoid(x)
def _layernorm(x, axis=-1, eps=1e-5):
    m = x.mean(dim=axis, keepdim=True)
    v = x.var(dim=axis, keepdim=True, unbiased=False)
    return (x - m) / torch.sqrt(v + eps)
def _scatter_add(tgt, idx, src, axis=0):
    return tgt.clone().index_add_(axis, idx, src)
def _segment_sum(src, seg, n):
    out = torch.zeros((n,) + src.shape[1:], dtype=src.dtype)
    return out.index_add_(0, seg, src)
def _pad(x, pw):
    flat = tuple(v for pair in reversed(pw) for v in pair)
    return torch.nn.functional.pad(x, flat)

kernel_name = 'concat_heads__gqa_block_cached__mha_block_cached__near_ridge'

def setup_inputs():

    _w = (lambda a: (lambda t: t.to(torch.bfloat16) if t.is_floating_point() else t)(torch.as_tensor(a)))
    def _A_setup():
        B, Sq, D = 16, 160, 512
        Hq, Hkv, Sk, Dh = 32, 32, 512, 128
        assert Hq % Hkv == 0, (Hq, Hkv)
        return {
            "x": _w((lambda *s: torch.randn(*s, dtype=torch.bfloat16) * 0.8)(B, Sq, D)),
            "k_cache": _w((lambda *s: torch.randn(*s, dtype=torch.bfloat16) * 0.8)(B, Hkv, Sk, Dh)),
            "v_cache": _w((lambda *s: torch.randn(*s, dtype=torch.bfloat16) * 0.05)(B, Hkv, Sk, Dh)),
            "wq": _w((lambda *s: torch.randn(*s, dtype=torch.bfloat16) * 0.8)(D, Hq * Dh) * (D ** -0.5)),
            "wo": _w((lambda *s: torch.randn(*s, dtype=torch.bfloat16) * 0.05)(Hq * Dh, D) * ((Hq * Dh) ** -0.5)),
        }
    _A_in = _A_setup()

    def _B_setup():
        B, Sq, D = 16, 160, 512
        H, Sk, Dh = 32, 512, 128
        return {
            "x": _w((lambda *s: torch.randn(*s, dtype=torch.bfloat16) * 0.8)(B, Sq, D)),
            "k_cache": _w((lambda *s: torch.randn(*s, dtype=torch.bfloat16) * 0.8)(B, H, Sk, Dh)),
            "v_cache": _w((lambda *s: torch.randn(*s, dtype=torch.bfloat16) * 0.05)(B, H, Sk, Dh)),
            "wq": _w((lambda *s: torch.randn(*s, dtype=torch.bfloat16) * 0.8)(D, H * Dh) * (D ** -0.5)),
            "wo": _w((lambda *s: torch.randn(*s, dtype=torch.bfloat16) * 0.05)(H * Dh, D) * ((H * Dh) ** -0.5)),
        }
    _B_in = _B_setup()

    return {
        "A_x": _A_in["x"],
        "A_k_cache": _A_in["k_cache"],
        "A_v_cache": _A_in["v_cache"],
        "A_wq": _A_in["wq"],
        "A_wo": _A_in["wo"],
        "B_x": _B_in["x"],
        "B_k_cache": _B_in["k_cache"],
        "B_v_cache": _B_in["v_cache"],
        "B_wq": _B_in["wq"],
        "B_wo": _B_in["wo"],
    }

def reference(A_x, A_k_cache, A_v_cache, A_wq, A_wo, B_x, B_k_cache, B_v_cache, B_wq, B_wo):
    def _A_ref(x, k_cache, v_cache, wq, wo):
        B, Sq, D = x.shape
        Hq, Dh = 32, 128
        Hkv = k_cache.shape[1]
        rep = Hq // Hkv
        q = (lambda x, *d: x.permute(*d))((x @ wq).reshape(B, Sq, Hq, Dh), 0, 2, 1, 3)
        k = (lambda x, n, axis: torch.repeat_interleave(x, n, dim=axis))(k_cache, rep, axis=1)
        v = (lambda x, n, axis: torch.repeat_interleave(x, n, dim=axis))(v_cache, rep, axis=1)
        scores = torch.einsum("bhqd,bhkd->bhqk", q, k) * (Dh ** -0.5)
        p = _softmax(scores, axis=-1)
        o = torch.einsum("bhqk,bhkd->bhqd", p, v)
        return (lambda x, *d: x.permute(*d))(o, 0, 2, 1, 3).reshape(B, Sq, Hq * Dh) @ wo

    def _B_ref(x, k_cache, v_cache, wq, wo):
        B, Sq, D = x.shape
        H, Dh = 32, 128
        q = (lambda x, *d: x.permute(*d))((x @ wq).reshape(B, Sq, H, Dh), 0, 2, 1, 3)
        scores = torch.einsum("bhqd,bhkd->bhqk", q, k_cache) * (Dh ** -0.5)
        p = _softmax(scores, axis=-1)
        o = torch.einsum("bhqk,bhkd->bhqd", p, v_cache)
        return (lambda x, *d: x.permute(*d))(o, 0, 2, 1, 3).reshape(B, Sq, H * Dh) @ wo

    a = _A_ref(A_x, A_k_cache, A_v_cache, A_wq, A_wo)
    b = _B_ref(B_x, B_k_cache, B_v_cache, B_wq, B_wo)
    return torch.concatenate([a, b], axis=-1)
```

```cpp
#include <hip/hip_runtime.h>
#include <hip/hip_bf16.h>
typedef unsigned short u16;
typedef unsigned long long u64;
typedef unsigned u32x4 __attribute__((ext_vector_type(4)));

__device__ __forceinline__ float bf2f(u16 v) { return __uint_as_float((unsigned)v << 16); }
__device__ __forceinline__ u16 f2bf(float f) { return __builtin_bit_cast(u16, (__bf16)f); }
__device__ __forceinline__ void wave_sync() {
  __builtin_amdgcn_fence(__ATOMIC_RELEASE, "wavefront"); __builtin_amdgcn_wave_barrier(); __builtin_amdgcn_fence(__ATOMIC_ACQUIRE, "wavefront");
}

typedef __attribute__((address_space(1))) unsigned gu32;
__device__ __forceinline__ void st_agent(unsigned* p, unsigned v) { __hip_atomic_store((gu32*)p, v, __ATOMIC_RELAXED, __HIP_MEMORY_SCOPE_AGENT); }
__device__ __forceinline__ unsigned ld_agent(const unsigned* p) { return __hip_atomic_load((gu32*)p, __ATOMIC_RELAXED, __HIP_MEMORY_SCOPE_AGENT); }

__device__ __forceinline__ void slow_reduce(u16* __restrict__ part, u16* __restrict__ y, int pair, int own, int wid, int lane) {
  unsigned* fl = (unsigned*)(part + 655360);
  const int i = lane < 7 ? lane : 0;
  unsigned* in = fl + ((pair * 8 + i) * 4 + wid) * 32; unsigned* to = fl + ((((pair + 1 + i) & 7) * 8 + i) * 4 + wid) * 32;
  const unsigned want = ld_agent(in + 16);
  for (unsigned n = 0; n < (1u << 20); ++n) {
    const bool ok = __all(ld_agent(in) == want);
    asm volatile("s_waitcnt vmcnt(0)" ::: "memory");
    if (lane < 7) st_agent(to, ld_agent(to + 16));
    if (ok) break;
    __builtin_amdgcn_s_sleep(32);
  }
  __builtin_amdgcn_fence(__ATOMIC_ACQUIRE, "agent");
#pragma nounroll
  for (int e = 0; e < 20; ++e) {
    const int j = e >> 2, dw = e & 3;
    float lo = 0.f, hi = 0.f;
    for (int s = 0; s < 8; ++s) { const unsigned v = ld_agent((unsigned*)(part + (size_t)own * 81920 + s * 10240 + wid * 2560 + j * 512 + lane * 8) + dw);
      lo += __uint_as_float(v << 16); hi += __uint_as_float(v & 0xffff0000u); }
    *(unsigned*)(y + ((wid & 1) * 80 + j * 16 + (lane & 15)) * 1024 + (wid >> 1) * 32 + (lane >> 4) * 8 + dw * 2) = (unsigned)f2bf(lo) | ((unsigned)f2bf(hi) << 16);
  }
}

__device__ __forceinline__ void slow_path(const u16* __restrict__ x, const u16* __restrict__ wq, const u16* __restrict__ k, const u16* __restrict__ v,
                                          const u16* __restrict__ wo, u16* __restrict__ part, int pair, int wid, float* sc, int lane) {
  float* qs = sc; float* ps = sc + 128; float* os = sc + 640;
#pragma nounroll
  for (int r = 0; r < 40; ++r) {
    const int row = wid * 40 + r;
#pragma nounroll
    for (int hi = 0; hi < 4; ++hi) {
      const int head = 4 * pair + hi;
      const u16* kh = k + (size_t)head * 65536; const u16* vh = v + (size_t)head * 65536;
      float q0 = 0.f, q1 = 0.f;
      for (int m = 0; m < 512; ++m) { const float xv = bf2f(x[row * 512 + m]); const u16* w = wq + (size_t)m * 4096 + head * 128;
        q0 = fmaf(xv, bf2f(w[lane]), q0); q1 = fmaf(xv, bf2f(w[lane + 64]), q1); }
      qs[lane] = bf2f(f2bf(q0)); qs[lane + 64] = bf2f(f2bf(q1));
      wave_sync();
      float mx = -INFINITY;
#pragma nounroll
      for (int i = 0; i < 8; ++i) { const int key = i * 64 + lane; float s = 0.f;
        for (int d = 0; d < 128; ++d) s = fmaf(qs[d], bf2f(kh[key * 128 + d]), s);
        s *= 0.08838834764831845f; ps[key] = s; mx = fmaxf(mx, s); }
      for (int o2 = 32; o2 >= 1; o2 >>= 1) mx = fmaxf(mx, __shfl_xor(mx, o2));
      float l = 0.f;
      for (int i = 0; i < 8; ++i) { const int key = i * 64 + lane; const float p = __expf(ps[key] - mx); ps[key] = p; l += p; }
      for (int o2 = 32; o2 >= 1; o2 >>= 1) l += __shfl_xor(l, o2);
      wave_sync();
      const float il = 1.f / l;
      float o0 = 0.f, o1 = 0.f;
      for (int key = 0; key < 512; ++key) { const float p = ps[key] * il; o0 = fmaf(p, bf2f(vh[key * 128 + lane]), o0); o1 = fmaf(p, bf2f(vh[key * 128 + 64 + lane]), o1); }
      os[hi * 128 + lane] = bf2f(f2bf(o0)); os[hi * 128 + 64 + lane] = bf2f(f2bf(o1));
      wave_sync();
    }
#pragma nounroll
    for (int c = 0; c < 8; ++c) {
      float acc = 0.f;
      for (int kk = 0; kk < 512; ++kk) acc = fmaf(os[kk], bf2f(wo[(size_t)(pair * 512 + kk) * 512 + c * 64 + lane]), acc);
      const int qh = row / 80, rr = row % 80;
      const unsigned mine = f2bf(acc), other = __shfl_down(mine, 1);
      if (!(lane & 1)) st_agent((unsigned*)(part + (size_t)c * 81920 + pair * 10240 + ((lane >> 5) * 2 + qh) * 2560 + (rr >> 4) * 512 + (((lane >> 3) & 3) * 16 + (rr & 15)) * 8 + (lane & 7)),
                                mine | (other << 16));
    }
    wave_sync();
  }
}

__global__ __launch_bounds__(256, 1) void fused_kernel(const u16*, const u16*, const u16*, const u16*, const u16*, const u16*, const u16*, const u16*, const u16*, const u16*, u16*, u16*) {
  extern __shared__ __attribute__((aligned(16))) char lds[];
  typedef u16* const __attribute__((address_space(4)))* kargs;
  kargs ka = (kargs)__builtin_amdgcn_kernarg_segment_ptr();
  int bid = blockIdx.x, wid = __builtin_amdgcn_readfirstlane(threadIdx.x >> 6);
  unsigned f0;
  asm volatile(
".macro SA d,x,y\ns_add_u32 \\d, \\x, \\y\n.endm\n.macro DL x,r,o,i\nbuffer_load_dwordx4 v[\\x], s[\\r:\\r+3], s[\\o] offen offset:\\i lds\n.endm\n"
".macro Ra d,x,o\nds_read_b128 a[\\d:\\d+3], v[\\x] offset:\\o\n.endm\n.macro Ta d,x,o\nds_read_b64_tr_b16 a[\\d:\\d+1], v[\\x] offset:\\o\n.endm\n"
".macro Zvaa d,x,y\nv_mfma_f32_16x16x32_bf16 v[\\d:\\d+3], a[\\x:\\x+3], a[\\y:\\y+3], 0\n.endm\n.macro AR d,x\nv_accvgpr_read_b32 v[\\d], a[\\x]\n.endm\n"
".macro MU d,x\nv_mul_f32 v[\\d], \\x, v[\\d]\n.endm\n.macro CV d,x,y\nv_cvt_pk_bf16_f32 v[\\d], v[\\x], v[\\y]\n.endm\n"
".macro Mvaa d,x,y\nv_mfma_f32_16x16x32_bf16 v[\\d:\\d+3], a[\\x:\\x+3], a[\\y:\\y+3], v[\\d:\\d+3]\n.endm\n.macro DN x,r,o,i\nbuffer_load_dwordx4 v[\\x], s[\\r:\\r+3], s[\\o] offen offset:\\i nt lds\n.endm\n"
".macro Zvav d,x,y\nv_mfma_f32_16x16x32_bf16 v[\\d:\\d+3], a[\\x:\\x+3], v[\\y:\\y+3], 0\n.endm\n.macro Mvav d,x,y\nv_mfma_f32_16x16x32_bf16 v[\\d:\\d+3], a[\\x:\\x+3], v[\\y:\\y+3], v[\\d:\\d+3]\n.endm\n"
".macro Cvav d,x,y,c\nv_mfma_f32_16x16x32_bf16 v[\\d:\\d+3], a[\\x:\\x+3], v[\\y:\\y+3], v[\\c:\\c+3]\n.endm\n.macro EX d\nv_exp_f32 v[\\d], v[\\d]\n.endm\n"
".macro Zaav d,x,y\nv_mfma_f32_16x16x32_bf16 a[\\d:\\d+3], a[\\x:\\x+3], v[\\y:\\y+3], 0\n.endm\n.macro Maav d,x,y\nv_mfma_f32_16x16x32_bf16 a[\\d:\\d+3], a[\\x:\\x+3], v[\\y:\\y+3], a[\\d:\\d+3]\n.endm\n"
".macro Zava d,x,y\nv_mfma_f32_16x16x32_bf16 a[\\d:\\d+3], v[\\x:\\x+3], a[\\y:\\y+3], 0\n.endm\n.macro Mava d,x,y\nv_mfma_f32_16x16x32_bf16 a[\\d:\\d+3], v[\\x:\\x+3], a[\\y:\\y+3], a[\\d:\\d+3]\n.endm\n"
".macro Maaa d,x,y\nv_mfma_f32_16x16x32_bf16 a[\\d:\\d+3], a[\\x:\\x+3], a[\\y:\\y+3], a[\\d:\\d+3]\n.endm\ns_bfe_u32 s19, %[bid], 0x10002\ns_mul_i32 s18, s19, 40\ns_load_dwordx8 s[8:15], %[ka], s18\n"
"SA s18,s18,32\ns_load_dwordx2 s[16:17], %[ka], s18\ns_load_dwordx4 s[20:23], %[ka], 0x50\ns_memtime s[88:89]\ns_memrealtime s[96:97]\ns_lshr_b32 s31, %[bid], 5\n"
"s_and_b32 s24, %[bid], 1\ns_bfe_u32 s18, %[bid], 0x20003\ns_lshl2_add_u32 s24, s24, s18\ns_bfe_u32 s30, %[bid], 0x10001\ns_lshl3_add_u32 s30, s30, s31\nSA s25,s31,s24\n"
"s_and_b32 s26, s25, 1\ns_lshl_b32 s26, s26, 2\ns_lshr_b32 s28, %[bid], 3\ns_lshl2_add_u32 s29, s28, %[wid]\nv_mbcnt_lo_u32_b32 v0, -1, 0\nv_mbcnt_hi_u32_b32 v0, -1, v0\n"
"v_lshlrev_b32 v253, 4, v0\nv_bfe_u32 v1, v0, 2, 3\nv_lshrrev_b32 v2, 5, v0\nv_and_b32 v3, 3, v0\nv_lshlrev_b32 v2, 6, v2\nv_lshl_add_u32 v2, v3, 4, v2\n"
"v_lshl_add_u32 v251, v1, 10, v2\nv_lshl_add_u32 v252, v1, 13, v2\ns_lshr_b32 s90, %[wid], 1\ns_and_b32 s91, %[wid], 1\ns_lshl1_add_u32 s62, s24, s90\ns_mul_i32 s60, %[wid], 40960\n"
"s_lshl_b32 s61, s62, 9\ns_lshl_b32 s92, s91, 7\nSA s61,s61,s92\ns_mul_i32 s65, %[wid], 5280\nSA s65,s65,%[lds]\ns_mul_i32 s66, %[wid], 4128\n"
"SA s66,s66,%[lds]\ns_lshl_b32 s93, s26, 19\ns_lshl_b32 s73, s26, 7\ns_mov_b32 s38, 163840\ns_mov_b32 s39, 0x20000\ns_mov_b32 s42, 4194304\n"
"s_mov_b32 s43, 0x20000\ns_mul_i32 s18, s30, 163840\ns_getpc_b64 s[94:95]\n2:\nv_lshrrev_b32 v255, 8, v253\ns_lshl_b32 s92, s29, 2\n"
"v_add_lshl_u32 v255, v255, s92, 7\nv_min_u32 v255, END%=-2b-4, v255\nglobal_load_dword v255, v255, s[94:95]\ns_waitcnt lgkmcnt(0)\nSA s36,s8,s18\ns_addc_u32 s37, s9, 0\n"
"s_and_b32 s37, s37, 0xffff\ns_mov_b32 s40, s14\ns_and_b32 s41, s15, 0xffff\nSA s74,s60,s73\nSA s70,s74,0\nSA m0,s65,1024\n"
"SA s71,s74,8192\nDL 251,36,70,0\nSA m0,s65,2080\nSA s70,s74,16384\nDL 251,36,71,0\nSA m0,s65,3136\n"
"SA s71,s74,24576\nDL 251,36,70,0\nSA m0,s65,4192\nSA s70,s74,32768\nDL 251,36,71,0\nSA m0,s65,5248\n"
"SA s75,s61,s93\nDL 251,36,70,0\nSA s70,s75,0\nSA m0,s66,109312\nSA s71,s75,65536\nDL 252,40,70,0\n"
"SA m0,s66,110344\nSA s70,s75,131072\nDL 252,40,71,0\nSA m0,s66,111376\nSA s71,s75,196608\nDL 252,40,70,0\n"
"SA m0,s66,112408\nSA s93,s93,262144\nDL 252,40,71,0\ns_and_b32 s93, s93, 4194303\nSA s75,s61,s93\nSA s70,s75,0\n"
"SA m0,s66,67072\nSA s71,s75,65536\nDL 252,40,70,0\nSA m0,s66,68104\nSA s70,s75,131072\nDL 252,40,71,0\n"
"SA m0,s66,69136\nSA s71,s75,196608\nDL 252,40,70,0\nSA m0,s66,70168\nSA s73,s73,128\nDL 252,40,71,0\n"
"s_and_b32 s73, s73, 1023\nSA s74,s60,s73\nSA s70,s74,0\nSA m0,s65,88192\nSA s71,s74,8192\nDL 251,36,70,0\n"
"SA m0,s65,89248\nSA s70,s74,16384\nDL 251,36,71,0\nSA m0,s65,90304\nSA s71,s74,24576\nDL 251,36,70,0\n"
"SA m0,s65,91360\nSA s70,s74,32768\nDL 251,36,71,0\nSA m0,s65,92416\nSA s93,s93,262144\nDL 251,36,70,0\n"
"s_waitcnt vmcnt(18)\ns_waitcnt lgkmcnt(0)\nv_mov_b32 v170, s88\nv_mov_b32 v171, s96\ns_and_b32 s25, s25, 7\ns_lshl1_add_u32 s27, s31, s24\n"
"s_mul_i32 s18, s28, 20\ns_lshr_b32 s18, s18, 8\ns_mul_i32 s18, s18, 13\ns_sub_u32 s18, s28, s18\ns_cmp_ge_u32 s28, 26\ns_cselect_b32 s18, 0, s18\n"
"s_lshl_b32 s28, 1, s18\ns_or_b32 s28, s28, 0x2000\ns_lshl_b32 s18, s30, 22\nSA s10,s10,s18\ns_addc_u32 s11, s11, 0\nSA s12,s12,s18\n"
"s_addc_u32 s13, s13, 0\ns_lshl4_add_u32 s18, s19, s30\ns_mul_i32 s18, s18, 1343488\nSA s20,s20,s18\ns_addc_u32 s21, s21, 0\ns_mul_i32 s18, s30, 327680\n"
"s_lshl3_add_u32 s19, s19, s25\ns_lshl_b32 s19, s19, 7\nSA s18,s18,s19\nSA s22,s22,s18\ns_addc_u32 s23, s23, 0\ns_lshl_b32 s63, s62, 18\n"
"s_lshl_b32 s92, s91, 12\nSA s62,s63,s92\ns_lshl_b32 s92, s91, 16\nSA s63,s63,s92\ns_mul_i32 s69, %[wid], 8256\nSA s69,s69,%[lds]\n"
"s_mul_i32 s79, s90, 1\nSA s79,s79,s27\ns_and_b32 s79, s79, 15\ns_lshl_b32 s79, s79, 13\ns_mov_b32 s80, 0\ns_mov_b32 s33, 0\n"
"s_mov_b32 s35, 100608\nv_bfe_u32 v1, v0, 2, 2\nv_lshrrev_b32 v2, 4, v0\nv_lshlrev_b32 v2, 6, v2\nv_lshl_add_u32 v2, v3, 4, v2\nv_lshl_add_u32 v250, v1, 8, v2\n"
"s_mov_b64 s[44:45], s[10:11]\ns_and_b32 s45, s45, 0xffff\ns_mov_b32 s46, 4194304\ns_mov_b32 s47, 0x20000\ns_mov_b64 s[48:49], s[12:13]\ns_and_b32 s49, s49, 0xffff\n"
"s_mov_b32 s50, 4194304\ns_mov_b32 s51, 0x20000\ns_mov_b64 s[52:53], s[16:17]\ns_and_b32 s53, s53, 0xffff\ns_mov_b32 s54, 4194304\ns_mov_b32 s55, 0x20000\n"
"s_mov_b64 s[56:57], s[20:21]\ns_and_b32 s57, s57, 0xffff\ns_mov_b32 s58, 1343488\ns_mov_b32 s59, 0x20000\ns_mul_i32 s67, %[wid], 4160\nSA s67,s67,130304\n"
"SA s67,s67,%[lds]\ns_mul_i32 s68, %[wid], 4224\nSA s68,s68,146944\nSA s68,s68,%[lds]\ns_lshl_b32 s92, s91, 5\ns_sub_u32 s67, s67, s92\n"
"s_lshl_b32 s92, s91, 6\ns_sub_u32 s68, s68, s92\ns_mul_i32 s64, s24, 20480\ns_mul_i32 s92, %[wid], 5120\nSA s64,s64,s92\nSA s78,s25,1\n"
"s_mul_i32 s82, s90, 8320\nSA s82,s82,130304\nSA s82,s82,%[lds]\ns_mul_i32 s83, s90, 8768\ns_mul_i32 s92, s90, 33024\ns_sub_u32 s84, s83, s92\n"
"SA s84,s84,16512\ns_mul_i32 s85, %[wid], 4096\nSA s85,s85,%[lds]\ns_xor_b32 s92, %[wid], 2\ns_mul_i32 s86, s92, 4096\nSA s86,s86,%[lds]\n"
"s_lshl_b32 s87, %[wid], 7\nSA s87,s87,1310720\ns_lshl_b32 s96, s24, 12\ns_mul_i32 s97, s25, 163840\ns_mul_i32 s92, %[wid], 5120\nSA s97,s97,s92\n"
"s_mul_i32 s98, s91, 163840\ns_lshl_b32 s92, s90, 6\nSA s98,s98,s92\ns_lshl_b32 s99, %[wid], 2\nSA s99,s99,%[lds]\ns_add_u32 s30, %[lds], 25472\n"
"s_mov_b64 s[88:89], 0\ns_mov_b32 s76, 0x3e0293ee\ns_mov_b32 s77, 0x5f800000\nv_lshrrev_b32 v4, 4, v0\nv_and_b32 v5, 15, v0\nv_bfe_u32 v6, v0, 2, 2\n"
"v_lshrrev_b32 v7, 3, v5\nv_mul_u32_u24 v7, 1056, v7\nv_and_b32 v8, 7, v5\nv_lshl_add_u32 v7, v8, 6, v7\nv_lshl_add_u32 v7, v4, 4, v7\ns_mul_i32 s92, s91, 10560\n"
"SA s92,s92,%[lds]\nv_add_u32 v245, s92, v7\nv_mul_u32_u24 v7, 1032, v4\nv_lshl_add_u32 v7, v6, 6, v7\nv_lshl_add_u32 v7, v3, 4, v7\ns_mul_i32 s92, s90, 8256\n"
"SA s92,s92,%[lds]\nv_add_u32 v246, s92, v7\nv_lshlrev_b32 v7, 11, v6\nv_lshrrev_b32 v8, 1, v6\nv_lshl_add_u32 v7, v8, 5, v7\nv_lshl_add_u32 v7, v3, 6, v7\n"
"v_lshl_add_u32 v7, v4, 4, v7\nv_add_u32 v247, s82, v7\nv_mul_u32_u24 v7, 2080, v4\nv_lshl_add_u32 v7, v6, 6, v7\nv_lshl_add_u32 v7, v3, 3, v7\ns_mul_i32 s92, s90, 8448\n"
"SA s92,s92,146944\nSA s92,s92,%[lds]\nv_add_u32 v248, s92, v7\ns_memrealtime s[94:95]\ns_waitcnt lgkmcnt(0)\ns_mov_b32 s72, s94\n"
"v_mov_b32 v7, 0x3f803f80\nv_cmp_eq_u32 vcc, 0, v3\ns_nop 1\nv_cndmask_b32 v7, 0, v7, vcc\nv_accvgpr_write_b32 a244, v7\nv_accvgpr_write_b32 a245, v7\n"
"v_accvgpr_write_b32 a246, v7\nv_accvgpr_write_b32 a247, v7\ns_waitcnt vmcnt(9)\ns_barrier\nRa 248,245,1024\nRa 252,245,3136\n"
"Ra 212,245,5248\nRa 216,245,7360\nRa 220,245,9472\ns_and_b32 s93, s93, 4194303\nSA s75,s61,s93\nSA s70,s75,0\n"
"SA m0,s66,50560\nSA s71,s75,65536\nDL 252,40,70,0\nSA m0,s66,51592\nSA s70,s75,131072\nDL 252,40,71,0\n"
"SA m0,s66,52624\nSA s71,s75,196608\nDL 252,40,70,0\nSA m0,s66,53656\nSA s93,s93,262144\nDL 252,40,71,0\n"
"s_and_b32 s93, s93, 4194303\nSA s73,s73,128\ns_and_b32 s73, s73, 1023\nv_add_u32 v246, 0x1ab00, v246\nTa 180,246,0\nTa 182,246,256\n"
"Ta 184,246,8\nTa 186,246,264\nTa 188,246,512\nTa 190,246,768\nTa 192,246,520\nTa 194,246,776\n"
"Ta 196,246,4128\nTa 198,246,4384\nTa 200,246,4136\nTa 202,246,4392\nTa 204,246,4640\nTa 206,246,4896\n"
"Ta 208,246,4648\nTa 210,246,4904\ns_waitcnt lgkmcnt(0)\ns_mov_b32 s90, 10\ns_memtime s[88:89]\ns_memrealtime s[94:95]\n"
"v_readfirstlane_b32 s92, v170\nv_readfirstlane_b32 s91, v171\ns_waitcnt lgkmcnt(0)\ns_sub_u32 s88, s88, s92\ns_sub_u32 s94, s94, s91\ns_mul_i32 s94, s94, 205\n"
"s_mul_i32 s88, s88, 10\ns_movk_i32 s18, 107\ns_cmp_lt_u32 s88, s94\ns_cselect_b32 s91, 2, 0\ns_cselect_b32 s18, 96, s18\nRND%=:\n"
"Zvaa 0,180,248\nSA s75,s61,s93\nSA s70,s75,0\nSA s71,s75,65536\nZvaa 8,180,252\nSA s74,s60,s73\n"
"SA s93,s93,262144\ns_and_b32 s93, s93, 4194303\nZvaa 16,180,212\nSA s73,s73,128\nv_add_u32 v246, 0xffff5b00, v246\ns_and_b32 s73, s73, 1023\n"
"Zvaa 24,180,216\nSA s80,s80,0x40000000\ns_cselect_b32 s81, s35, -33536\nAR 240,160\nv_cmp_ngt_f32 vcc, s77, v240\nZvaa 32,180,220\n"
"SA s33,s33,0x40000000\ns_cselect_b32 s34, s35, -33536\nAR 160,0\nAR 161,1\nZvaa 36,184,220\ns_or_b64 s[88:89], s[88:89], vcc\n"
"v_cmp_nle_f32 vcc, 0.5, v240\nAR 180,40\nZvaa 28,184,216\ns_or_b64 s[88:89], s[88:89], vcc\nv_rcp_f32 v240, v240\nZvaa 20,184,212\n"
"AR 181,41\nMU 160,v240\nZvaa 12,184,252\nMU 161,v240\nAR 162,2\nZvaa 4,184,248\n"
"s_waitcnt vmcnt(9)\nAR 163,3\nMU 180,v240\ns_barrier\nZvaa 40,188,248\nSA m0,s66,109312\n"
"Ra 224,245,1536\nMU 181,v240\nZvaa 48,188,252\nDL 252,40,70,0\nZvaa 56,188,212\nSA m0,s66,110344\n"
"SA s70,s75,131072\nTa 180,246,0\nZvaa 64,188,216\nTa 182,246,256\nAR 182,42\nZvaa 72,188,220\n"
"Ra 228,245,3648\nAR 183,43\nZvaa 76,192,220\nDL 252,40,71,0\nZvaa 68,192,216\nSA m0,s66,111376\n"
"SA s71,s75,196608\nSA s75,s61,s93\nZvaa 60,192,212\nSA s93,s93,262144\ns_and_b32 s93, s93, 4194303\nRa 232,245,5760\n"
"Zvaa 52,192,252\nRa 236,245,7872\nAR 241,164\nZvaa 44,192,248\nDL 252,40,70,0\nZvaa 80,196,248\n"
"SA m0,s66,112408\nSA s70,s74,0\nRa 240,245,9984\nZvaa 88,196,252\nTa 184,246,8\nv_add_u32 v245, 0x15880, v245\n"
"Zvaa 96,196,212\nTa 186,246,264\nMU 162,v240\nZvaa 104,196,216\nDL 252,40,71,0\nZvaa 112,196,220\n"
"SA m0,s65,22144\nSA s71,s74,8192\nTa 188,246,512\nZvaa 116,200,220\nTa 190,246,768\nMU 163,v240\n"
"Zvaa 108,200,216\nTa 192,246,520\nCV 160,160,161\nZvaa 100,200,212\nDL 251,36,70,0\nZvaa 92,200,252\n"
"SA m0,s65,23200\nSA s70,s74,16384\nTa 194,246,776\nZvaa 84,200,248\nTa 196,246,4128\nMU 182,v240\n"
"Zvaa 120,204,248\nTa 198,246,4384\nMU 183,v240\nZvaa 128,204,252\nDL 251,36,71,0\nZvaa 136,204,212\n"
"SA m0,s65,24256\nSA s71,s74,24576\nTa 200,246,4136\nZvaa 144,204,216\nTa 202,246,4392\nCV 180,180,181\n"
"Zvaa 152,204,220\nTa 204,246,4640\nv_cmp_ngt_f32 vcc, s77, v241\nZvaa 156,208,220\nDL 251,36,70,0\nZvaa 148,208,216\n"
"SA m0,s65,25312\nSA s70,s74,32768\nTa 206,246,4896\nZvaa 140,208,212\nSA s74,s60,s73\nCV 161,162,163\n"
"CV 181,182,183\nZvaa 132,208,252\nSA s73,s73,128\nAR 200,80\nAR 201,81\nZvaa 124,208,248\n"
"s_waitcnt lgkmcnt(5)\nTa 208,246,4648\nAR 220,120\nMvaa 0,180,224\nDL 251,36,71,0\nMvaa 8,180,228\n"
"SA m0,s65,26368\nSA s71,s75,65536\nTa 210,246,4904\nMvaa 16,180,232\ns_and_b32 s73, s73, 1023\nv_add_u32 v246, 0xffffbf80, v246\n"
"AR 221,121\nMvaa 24,180,236\ns_or_b64 s[88:89], s[88:89], vcc\nAR 162,4\nAR 163,5\nMvaa 32,180,240\n"
"DL 251,36,70,0\nMvaa 36,184,240\nSA s70,s75,0\nAR 182,44\nAR 183,45\nMvaa 28,184,236\n"
"MU 200,v240\nMU 201,v240\nMvaa 20,184,232\nAR 202,82\nAR 203,83\nMvaa 12,184,228\n"
"MU 220,v240\nMU 221,v240\nMvaa 4,184,224\ns_waitcnt vmcnt(9) lgkmcnt(0)\nAR 222,122\nAR 223,123\n"
"s_barrier\ns_cmp_lg_u32 s91, 0\ns_cbranch_scc1 1f\ns_sleep 10\n1:\nMvaa 40,188,224\n"
"SA m0,s66,1024\nRa 248,245,0\nv_cmp_nle_f32 vcc, 0.5, v241\nMvaa 48,188,228\nDL 252,40,70,0\nMvaa 56,188,232\n"
"SA m0,s66,2056\nSA s70,s75,131072\nTa 180,246,0\nMvaa 64,188,236\ns_or_b64 s[88:89], s[88:89], vcc\nTa 182,246,256\n"
"MU 162,v240\nMvaa 72,188,240\nRa 252,245,2112\nMU 163,v240\nMvaa 76,192,240\nDL 252,40,71,0\n"
"Mvaa 68,192,236\nSA m0,s66,3088\nSA s71,s75,196608\nSA s75,s61,s93\nMvaa 60,192,232\nSA s93,s93,262144\n"
"s_and_b32 s93, s93, 4194303\nRa 212,245,4224\nMvaa 52,192,228\nRa 216,245,6336\nMU 182,v240\nMvaa 44,192,224\n"
"DL 252,40,70,0\nMvaa 80,196,224\nSA m0,s66,4120\nSA s70,s75,0\nRa 220,245,8448\nMvaa 88,196,228\n"
"Ta 184,246,8\nMU 183,v240\nMvaa 96,196,232\nTa 186,246,264\nMU 202,v240\nMvaa 104,196,236\n"
"DL 252,40,71,0\nMvaa 112,196,240\nSA s71,s75,65536\nTa 188,246,512\nMU 203,v240\nMvaa 116,200,240\n"
"Ta 190,246,768\nCV 200,200,201\nMvaa 108,200,236\nTa 192,246,520\nMU 222,v240\nMvaa 100,200,232\n"
"Ta 194,246,776\nMU 223,v240\nMvaa 92,200,228\nTa 196,246,4128\nCV 220,220,221\nMvaa 84,200,224\n"
"Ta 198,246,4384\nCV 162,162,163\nMvaa 120,204,224\nTa 200,246,4136\nCV 182,182,183\nMvaa 128,204,228\n"
"Ta 202,246,4392\nCV 201,202,203\nMvaa 136,204,232\nv_rcp_f32 v241, v241\nMvaa 144,204,236\nCV 221,222,223\n"
"AR 164,8\nMvaa 152,204,240\nTa 204,246,4640\nAR 165,9\nMvaa 156,208,240\nTa 206,246,4896\n"
"AR 184,48\nMvaa 148,208,236\nAR 185,49\nAR 254,7\nMvaa 140,208,232\nAR 163,6\n"
"AR 255,47\nMvaa 132,208,228\nAR 183,46\nAR 202,84\nMvaa 124,208,224\ns_waitcnt lgkmcnt(4)\n"
"Ta 208,246,4648\nAR 203,85\nMvaa 0,180,248\nTa 210,246,4904\nv_add_u32 v246, 0xe580, v246\nMvaa 8,180,252\n"
"AR 222,124\nAR 223,125\nMvaa 16,180,212\nMU 164,v241\nMU 165,v241\nMvaa 24,180,216\n"
"AR 166,10\nAR 167,11\nMvaa 32,180,220\nMU 184,v241\nMU 185,v241\nMvaa 36,184,220\n"
"AR 186,50\nAR 187,51\nMvaa 28,184,216\nAR 242,168\nMU 254,v240\nMvaa 20,184,212\n"
"MU 163,v240\nMU 255,v240\nMvaa 12,184,252\nMU 183,v240\nMU 202,v240\nMvaa 4,184,248\n"
"s_waitcnt vmcnt(9) lgkmcnt(0)\nMU 203,v240\nMU 222,v240\ns_barrier\ns_cmp_lg_u32 s91, 0\ns_cbranch_scc1 1f\n"
"s_sleep 8\n1:\nMvaa 40,188,248\nSA m0,s66,50560\nRa 224,245,512\nMU 223,v240\n"
"Mvaa 48,188,252\nDL 252,40,70,0\nMvaa 56,188,212\nSA m0,s66,51592\nSA s70,s75,131072\nTa 180,246,0\n"
"Mvaa 64,188,216\nTa 182,246,256\nMU 166,v241\nMvaa 72,188,220\nRa 228,245,2624\nMU 167,v241\n"
"Mvaa 76,192,220\nDL 252,40,71,0\nMvaa 68,192,216\nSA m0,s66,52624\nSA s71,s75,196608\nSA s75,s61,s93\n"
"Mvaa 60,192,212\nSA s93,s93,262144\ns_and_b32 s93, s93, 4194303\nRa 232,245,4736\nMvaa 52,192,252\nRa 236,245,6848\n"
"CV 164,164,165\nMvaa 44,192,248\nDL 252,40,70,0\nMvaa 80,196,248\nSA m0,s66,53656\nSA s70,s74,0\n"
"Ra 240,245,8960\nMvaa 88,196,252\nTa 184,246,8\nv_add_u32 v245, 0xfffefe00, v245\nMvaa 96,196,212\nTa 186,246,264\n"
"MU 186,v241\nMvaa 104,196,216\nDL 252,40,71,0\nMvaa 112,196,220\nSA m0,s65,67072\nSA s71,s74,8192\n"
"Ta 188,246,512\nMvaa 116,200,220\nTa 190,246,768\nMU 187,v241\nMvaa 108,200,216\nTa 192,246,520\n"
"CV 184,184,185\nMvaa 100,200,212\nDL 251,36,70,0\nMvaa 92,200,252\nSA m0,s65,68128\nSA s70,s74,16384\n"
"Ta 194,246,776\nMvaa 84,200,248\nTa 196,246,4128\nv_cmp_ngt_f32 vcc, s77, v242\nMvaa 120,204,248\nTa 198,246,4384\n"
"CV 163,163,254\nMvaa 128,204,252\nDL 251,36,71,0\nMvaa 136,204,212\nSA m0,s65,69184\nSA s71,s74,24576\n"
"Ta 200,246,4136\nMvaa 144,204,216\ns_or_b64 s[88:89], s[88:89], vcc\nTa 202,246,4392\nCV 183,183,255\nMvaa 152,204,220\n"
"Ta 204,246,4640\nCV 202,202,203\nMvaa 156,208,220\nDL 251,36,70,0\nMvaa 148,208,216\nSA m0,s65,70240\n"
"SA s70,s74,32768\nTa 206,246,4896\nMvaa 140,208,212\nSA s74,s60,s73\nCV 222,222,223\nCV 165,166,167\n"
"Mvaa 132,208,252\nSA s73,s73,128\nCV 185,186,187\nAR 204,88\nMvaa 124,208,248\ns_waitcnt lgkmcnt(5)\n"
"Ta 208,246,4648\nAR 205,89\nMvaa 0,180,224\nDL 251,36,71,0\nMvaa 8,180,228\nSA m0,s65,71296\n"
"SA s71,s75,65536\nTa 210,246,4904\nMvaa 16,180,232\ns_and_b32 s73, s73, 1023\nv_add_u32 v246, 0xfffe5900, v246\nAR 224,128\n"
"Mvaa 24,180,236\nAR 225,129\nAR 254,87\nMvaa 32,180,240\nDL 251,36,70,0\nMvaa 36,184,240\n"
"SA s70,s75,0\nAR 203,86\nAR 255,127\nMvaa 28,184,236\nAR 223,126\nAR 166,12\n"
"Mvaa 20,184,232\nAR 167,13\nAR 186,52\nMvaa 12,184,228\nAR 187,53\nMU 204,v241\n"
"Mvaa 4,184,224\ns_waitcnt vmcnt(9) lgkmcnt(0)\nMU 205,v241\nAR 206,90\ns_barrier\ns_cmp_lg_u32 s91, 0\n"
"s_cbranch_scc1 1f\ns_sleep 4\n1:\nMvaa 40,188,224\nSA m0,s66,109312\nRa 248,245,0\n"
"AR 207,91\nMvaa 48,188,228\nDL 252,40,70,0\nMvaa 56,188,232\nSA m0,s66,110344\nSA s70,s75,131072\n"
"Ta 180,246,0\nMvaa 64,188,236\nTa 182,246,256\nMU 224,v241\nMvaa 72,188,240\nRa 252,245,2112\n"
"MU 225,v241\nMvaa 76,192,240\nDL 252,40,71,0\nMvaa 68,192,236\nSA m0,s66,111376\nSA s71,s75,196608\n"
"SA s75,s61,s93\nMvaa 60,192,232\nSA s93,s93,262144\ns_and_b32 s93, s93, 4194303\nRa 212,245,4224\nMvaa 52,192,228\n"
"Ra 216,245,6336\nAR 226,130\nMvaa 44,192,224\nDL 252,40,70,0\nMvaa 80,196,224\nSA m0,s66,112408\n"
"SA s70,s75,0\nRa 220,245,8448\nMvaa 88,196,228\nTa 184,246,8\nAR 227,131\nMvaa 96,196,232\n"
"Ta 186,246,264\nv_cmp_nle_f32 vcc, 0.5, v242\nMvaa 104,196,236\nDL 252,40,71,0\nMvaa 112,196,240\nSA s71,s75,65536\n"
"Ta 188,246,512\nMU 254,v240\nMvaa 116,200,240\ns_or_b64 s[88:89], s[88:89], vcc\nTa 190,246,768\nMU 203,v240\n"
"Mvaa 108,200,236\nTa 192,246,520\nMU 255,v240\nMvaa 100,200,232\nTa 194,246,776\nMU 223,v240\n"
"Mvaa 92,200,228\nTa 196,246,4128\nMU 166,v241\nMvaa 84,200,224\nTa 198,246,4384\nMU 167,v241\n"
"Mvaa 120,204,224\nTa 200,246,4136\nMU 186,v241\nMvaa 128,204,228\nTa 202,246,4392\nMU 187,v241\n"
"Mvaa 136,204,232\nMU 206,v241\nMU 207,v241\nMvaa 144,204,236\nCV 204,204,205\nMU 226,v241\n"
"Mvaa 152,204,240\nTa 204,246,4640\nMU 227,v241\nMvaa 156,208,240\nTa 206,246,4896\nCV 224,224,225\n"
"Mvaa 148,208,236\nv_rcp_f32 v242, v242\nMvaa 140,208,232\nCV 203,203,254\nCV 223,223,255\nMvaa 132,208,228\n"
"CV 166,166,167\nCV 186,186,187\nMvaa 124,208,224\ns_waitcnt lgkmcnt(4)\nTa 208,246,4648\nCV 205,206,207\n"
"Mvaa 0,180,248\nTa 210,246,4904\nCV 225,226,227\nMvaa 8,180,252\nAR 168,16\nAR 169,17\n"
"Mvaa 16,180,212\nAR 188,56\nAR 189,57\nMvaa 24,180,216\nAR 254,15\nAR 167,14\n"
"Mvaa 32,180,220\nAR 255,55\nAR 187,54\nMvaa 36,184,220\nAR 206,92\nAR 207,93\n"
"Mvaa 28,184,216\nAR 226,132\nAR 227,133\nMvaa 20,184,212\nMU 168,v242\nMU 169,v242\n"
"Mvaa 12,184,252\nAR 170,18\nAR 171,19\nMvaa 4,184,248\ns_waitcnt vmcnt(9) lgkmcnt(0)\nMU 188,v242\n"
"MU 189,v242\ns_barrier\nMvaa 40,188,248\nSA m0,s66,1024\nRa 224,245,512\nAR 190,58\n"
"Mvaa 48,188,252\nDL 252,40,70,0\nMvaa 56,188,212\nSA m0,s66,2056\nSA s70,s75,131072\nTa 180,246,49536\n"
"Mvaa 64,188,216\nTa 182,246,49792\nAR 191,59\nMvaa 72,188,220\nRa 228,245,2624\nAR 243,172\n"
"Mvaa 76,192,220\nDL 252,40,71,0\nMvaa 68,192,216\nSA m0,s66,3088\nSA s71,s75,196608\nSA s75,s61,s93\n"
"Mvaa 60,192,212\nSA s93,s93,262144\ns_and_b32 s93, s93, 4194303\nRa 232,245,4736\nMvaa 52,192,252\nRa 236,245,6848\n"
"MU 254,v241\nMvaa 44,192,248\nDL 252,40,70,0\nMvaa 80,196,248\nSA m0,s66,4120\nSA s70,s74,0\n"
"Ra 240,245,8960\nMvaa 88,196,252\nTa 184,246,49544\nv_add_u32 v245, 0xaf80, v245\nMvaa 96,196,212\nTa 186,246,49800\n"
"MU 167,v241\nMvaa 104,196,216\nDL 252,40,71,0\nMvaa 112,196,220\nSA m0,s65,88192\nSA s71,s74,8192\n"
"Ta 188,246,50048\nMvaa 116,200,220\nTa 190,246,50304\nMU 255,v241\nMvaa 108,200,216\nTa 192,246,50056\n"
"MU 187,v241\nMvaa 100,200,212\nDL 251,36,70,0\nMvaa 92,200,252\nSA m0,s65,89248\nSA s70,s74,16384\n"
"Ta 194,246,50312\nMvaa 84,200,248\nTa 196,246,53664\nMU 206,v241\nMvaa 120,204,248\nTa 198,246,53920\n"
"MU 207,v241\nMvaa 128,204,252\nDL 251,36,71,0\nMvaa 136,204,212\nSA m0,s65,90304\nSA s71,s74,24576\n"
"Ta 200,246,53672\nMvaa 144,204,216\nTa 202,246,53928\nMU 226,v241\nMvaa 152,204,220\nTa 204,246,54176\n"
"MU 227,v241\nMvaa 156,208,220\nDL 251,36,70,0\nMvaa 148,208,216\nSA m0,s65,91360\nSA s70,s74,32768\n"
"Ta 206,246,54432\nMvaa 140,208,212\nSA s74,s60,s73\nMU 170,v242\nMU 171,v242\nMvaa 132,208,252\n"
"SA s73,s73,128\nCV 168,168,169\nMU 190,v242\nMvaa 124,208,248\ns_waitcnt lgkmcnt(5)\nTa 208,246,54184\n"
"MU 191,v242\nMvaa 0,180,224\nDL 251,36,71,0\nMvaa 8,180,228\nSA m0,s65,92416\nSA s71,s75,65536\n"
"Ta 210,246,54440\nMvaa 16,180,232\ns_and_b32 s73, s73, 1023\nv_add_u32 v246, 0x1a700, v246\nCV 188,188,189\nMvaa 24,180,236\n"
"v_cmp_ngt_f32 vcc, s77, v243\nCV 167,167,254\nMvaa 32,180,240\nDL 251,36,70,0\nMvaa 36,184,240\nSA s70,s75,0\n"
"CV 187,187,255\nCV 206,206,207\nMvaa 28,184,236\ns_or_b64 s[88:89], s[88:89], vcc\nCV 226,226,227\nCV 169,170,171\n"
"Mvaa 20,184,232\nCV 189,190,191\nAR 208,96\nMvaa 12,184,228\nAR 209,97\nAR 228,136\n"
"Mvaa 4,184,224\ns_waitcnt vmcnt(9) lgkmcnt(0)\nAR 229,137\nAR 254,95\ns_barrier\nMvaa 40,188,224\n"
"SA m0,s66,50560\nRa 248,245,0\nAR 207,94\nMvaa 48,188,228\nDL 252,40,70,0\nMvaa 56,188,232\n"
"SA m0,s66,51592\nSA s70,s75,131072\nTa 180,246,0\nMvaa 64,188,236\nTa 182,246,256\nAR 255,135\n"
"Mvaa 72,188,240\nRa 252,245,2112\nAR 227,134\nMvaa 76,192,240\nDL 252,40,71,0\nMvaa 68,192,236\n"
"SA m0,s66,52624\nSA s71,s75,196608\nSA s75,s61,s93\nMvaa 60,192,232\nSA s93,s93,262144\ns_and_b32 s93, s93, 4194303\n"
"Ra 212,245,4224\nMvaa 52,192,228\nRa 216,245,6336\nAR 170,20\nMvaa 44,192,224\nDL 252,40,70,0\n"
"Mvaa 80,196,224\nSA m0,s66,53656\nSA s70,s62,s79\nRa 220,245,8448\nMvaa 88,196,228\nTa 184,246,8\n"
"AR 171,21\nMvaa 96,196,232\nTa 186,246,264\nAR 190,60\nMvaa 104,196,236\nDL 252,40,71,0\n"
"Mvaa 112,196,240\nSA m0,s67,0\nSA s71,s75,65536\nTa 188,246,512\nMvaa 116,200,240\nSA s67,s67,s81\n"
"Ta 190,246,768\nAR 191,61\nMvaa 108,200,236\nTa 192,246,520\nMU 208,v242\nMvaa 100,200,232\n"
"DN 250,44,70,0\nMvaa 92,200,228\nTa 194,246,776\nMU 209,v242\nMvaa 84,200,224\nTa 196,246,4128\n"
"AR 210,98\nMvaa 120,204,224\nTa 198,246,4384\nAR 211,99\nMvaa 128,204,228\nDN 250,44,70,1024\n"
"Mvaa 136,204,232\nTa 200,246,4136\nMU 228,v242\nMvaa 144,204,236\nTa 202,246,4392\nMU 229,v242\n"
"Mvaa 152,204,240\nTa 204,246,4640\nAR 230,138\nMvaa 156,208,240\nDN 250,44,70,2048\nMvaa 148,208,236\n"
"Ta 206,246,4896\nAR 231,139\nMvaa 140,208,232\nv_cmp_nle_f32 vcc, 0.5, v243\nMU 254,v241\nMvaa 132,208,228\n"
"s_or_b64 s[88:89], s[88:89], vcc\nMU 207,v241\nMU 255,v241\nMvaa 124,208,224\ns_waitcnt lgkmcnt(5)\nTa 208,246,4648\n"
"MU 227,v241\nMvaa 0,180,248\nDN 250,44,70,3072\nMvaa 8,180,252\nSA s70,s75,0\nTa 210,246,4904\n"
"v_add_u32 v246, 0xfffe5900, v246\nMvaa 16,180,212\nMU 170,v242\nMU 171,v242\nMvaa 24,180,216\nMU 190,v242\n"
"MU 191,v242\nMvaa 32,180,220\nMU 210,v242\nMU 211,v242\nMvaa 36,184,220\nCV 208,208,209\n"
"MU 230,v242\nMvaa 28,184,216\nMU 231,v242\nCV 228,228,229\nMvaa 20,184,212\nv_rcp_f32 v243, v243\n"
"Mvaa 12,184,252\nCV 207,207,254\nCV 227,227,255\nMvaa 4,184,248\ns_waitcnt vmcnt(13) lgkmcnt(0)\nCV 170,170,171\n"
"CV 190,190,191\ns_barrier\nMvaa 40,188,248\nSA m0,s66,109312\nRa 224,245,512\nCV 209,210,211\n"
"Mvaa 48,188,252\nDL 252,40,70,0\nMvaa 56,188,212\nSA m0,s66,110344\nSA s70,s75,131072\nTa 180,246,0\n"
"Mvaa 64,188,216\nTa 182,246,256\nCV 229,230,231\nMvaa 72,188,220\nRa 228,245,2624\nAR 172,24\n"
"Mvaa 76,192,220\nDL 252,40,71,0\nMvaa 68,192,216\nSA m0,s66,111376\nSA s71,s75,196608\nSA s75,s61,s93\n"
"Mvaa 60,192,212\nSA s93,s93,262144\ns_and_b32 s93, s93, 4194303\nRa 232,245,4736\nMvaa 52,192,252\nRa 236,245,6848\n"
"AR 173,25\nMvaa 44,192,248\nDL 252,40,70,0\nMvaa 80,196,248\nSA m0,s66,112408\nSA s70,s74,0\n"
"Ra 240,245,8960\nMvaa 88,196,252\nTa 184,246,8\nAR 192,64\nMvaa 96,196,212\nTa 186,246,264\n"
"AR 193,65\nMvaa 104,196,216\nDL 252,40,71,0\nMvaa 112,196,220\nSA m0,s65,17536\nSA s71,s74,8192\n"
"Ta 188,246,512\nMvaa 116,200,220\nTa 190,246,768\nAR 254,23\nMvaa 108,200,216\nTa 192,246,520\n"
"AR 171,22\nMvaa 100,200,212\nDL 251,36,70,0\nMvaa 92,200,252\nSA m0,s65,18592\nSA s70,s74,16384\n"
"Ta 194,246,776\nMvaa 84,200,248\nTa 196,246,4128\nAR 255,63\nMvaa 120,204,248\nTa 198,246,4384\n"
"AR 191,62\nMvaa 128,204,252\nDL 251,36,71,0\nMvaa 136,204,212\nSA m0,s65,19648\nSA s71,s74,24576\n"
"Ta 200,246,4136\nMvaa 144,204,216\nTa 202,246,4392\nAR 210,100\nMvaa 152,204,220\nTa 204,246,4640\n"
"AR 211,101\nMvaa 156,208,220\nDL 251,36,70,0\nMvaa 148,208,216\nSA m0,s65,20704\nSA s70,s74,32768\n"
"Ta 206,246,4896\nMvaa 140,208,212\nSA s74,s60,s73\nAR 230,140\nAR 231,141\nMvaa 132,208,252\n"
"SA s73,s73,128\nMU 172,v243\nMU 173,v243\nMvaa 124,208,248\ns_waitcnt lgkmcnt(5)\nTa 208,246,4648\n"
"AR 174,26\nMvaa 0,180,224\nDL 251,36,71,0\nMvaa 8,180,228\nSA m0,s65,21760\nSA s71,s75,65536\n"
"Ta 210,246,4904\nMvaa 16,180,232\ns_and_b32 s73, s73, 1023\nAR 175,27\nMU 192,v243\nMvaa 24,180,236\n"
"MU 193,v243\nAR 194,66\nMvaa 32,180,240\nDL 251,36,70,0\nMvaa 36,184,240\nSA s70,s75,0\n"
"AR 195,67\nAR 244,176\nMvaa 28,184,236\nMU 254,v242\nMU 171,v242\nMvaa 20,184,232\n"
"MU 255,v242\nMU 191,v242\nMvaa 12,184,228\nMU 210,v242\nMU 211,v242\nMvaa 4,184,224\n"
"s_waitcnt vmcnt(13) lgkmcnt(0)\nMU 230,v242\nMU 231,v242\ns_barrier\nMvaa 40,188,224\nSA m0,s66,1024\n"
"Ra 248,245,21120\nMU 174,v243\nMvaa 48,188,228\nDL 252,40,70,0\nMvaa 56,188,232\nSA m0,s66,2056\n"
"SA s70,s75,131072\nTa 180,246,49536\nMvaa 64,188,236\nTa 182,246,49792\nMU 175,v243\nMvaa 72,188,240\n"
"Ra 252,245,23232\nCV 172,172,173\nMvaa 76,192,240\nDL 252,40,71,0\nMvaa 68,192,236\nSA m0,s66,3088\n"
"SA s71,s75,196608\nSA s75,s61,s93\nMvaa 60,192,232\nSA s93,s93,262144\ns_and_b32 s93, s93, 4194303\nRa 212,245,25344\n"
"Mvaa 52,192,228\nRa 216,245,27456\nMU 194,v243\nMvaa 44,192,224\nDL 252,40,70,0\nMvaa 80,196,224\n"
"SA m0,s66,4120\nSA s70,s62,s79\nRa 220,245,29568\nMvaa 88,196,228\nSA s79,s79,8192\nTa 184,246,49544\n"
"MU 195,v243\nMvaa 96,196,232\ns_and_b32 s79, s79, 0x1ffff\nTa 186,246,49800\nCV 192,192,193\nMvaa 104,196,236\n"
"DL 252,40,71,0\nMvaa 112,196,240\nSA m0,s68,0\nSA s71,s75,65536\nTa 188,246,50048\nMvaa 116,200,240\n"
"Ta 190,246,50304\nv_cmp_ngt_f32 vcc, s77, v244\nMvaa 108,200,236\nTa 192,246,50056\nCV 171,171,254\nMvaa 100,200,232\n"
"DN 250,48,70,0\nMvaa 92,200,228\ns_or_b64 s[88:89], s[88:89], vcc\nTa 194,246,50312\nCV 191,191,255\nMvaa 84,200,224\n"
"Ta 196,246,53664\nCV 210,210,211\nMvaa 120,204,224\nTa 198,246,53920\nCV 230,230,231\nMvaa 128,204,228\n"
"DN 250,48,70,1024\nMvaa 136,204,232\nSA m0,s68,32\nTa 200,246,53672\nCV 173,174,175\nMvaa 144,204,236\n"
"SA s68,s68,s81\nTa 202,246,53928\nCV 193,194,195\nMvaa 152,204,240\nSA s80,s80,0x40000000\ns_cselect_b32 s81, s35, -33536\n"
"Ta 204,246,54176\nAR 212,104\nMvaa 156,208,240\nDN 250,48,70,2048\nMvaa 148,208,236\nTa 206,246,54432\n"
"AR 213,105\nMvaa 140,208,232\nAR 232,144\nAR 233,145\nMvaa 132,208,228\nAR 254,103\n"
"AR 211,102\nMvaa 124,208,224\ns_waitcnt lgkmcnt(5)\nTa 208,246,54184\nAR 255,143\nMvaa 0,180,248\n"
"DN 250,48,70,3072\nMvaa 8,180,252\nSA s70,s75,0\nTa 210,246,54440\nv_add_u32 v246, 0x1a700, v246\nMvaa 16,180,212\n"
"AR 231,142\nAR 174,28\nMvaa 24,180,216\nAR 175,29\nAR 194,68\nMvaa 32,180,220\n"
"AR 195,69\nMU 212,v243\nMvaa 36,184,220\nMU 213,v243\nAR 214,106\nMvaa 28,184,216\n"
"AR 215,107\nMU 232,v243\nMvaa 20,184,212\nMU 233,v243\nAR 234,146\nMvaa 12,184,252\n"
"AR 235,147\nv_cmp_nle_f32 vcc, 0.5, v244\nMvaa 4,184,248\ns_waitcnt vmcnt(13) lgkmcnt(0)\nMU 254,v242\nMU 211,v242\n"
"s_barrier\nMvaa 40,188,248\nSA m0,s66,38656\nRa 224,245,21632\nMU 255,v242\nMvaa 48,188,252\n"
"DL 252,40,70,0\nMvaa 56,188,212\nSA m0,s66,39688\nSA s70,s75,131072\nTa 180,246,0\nMvaa 64,188,216\n"
"s_or_b64 s[88:89], s[88:89], vcc\nTa 182,246,256\nMU 231,v242\nMvaa 72,188,220\nRa 228,245,23744\nMU 174,v243\n"
"Mvaa 76,192,220\nDL 252,40,71,0\nMvaa 68,192,216\nSA m0,s66,40720\nSA s71,s75,196608\nSA s75,s61,s93\n"
"Mvaa 60,192,212\nSA s93,s93,262144\ns_and_b32 s93, s93, 4194303\nRa 232,245,25856\nMvaa 52,192,252\nRa 236,245,27968\n"
"MU 175,v243\nMvaa 44,192,248\nDL 252,40,70,0\nMvaa 80,196,248\nSA m0,s66,41752\nSA s70,s74,0\n"
"Ra 240,245,30080\nMvaa 88,196,252\nTa 184,246,8\nv_add_u32 v245, 0xffff3e80, v245\nMvaa 96,196,212\nTa 186,246,264\n"
"MU 194,v243\nMvaa 104,196,216\nDL 252,40,71,0\nMvaa 112,196,220\nSA m0,s65,55168\nSA s71,s74,8192\n"
"Ta 188,246,512\nMvaa 116,200,220\nTa 190,246,768\nMU 195,v243\nMvaa 108,200,216\nTa 192,246,520\n"
"MU 214,v243\nMvaa 100,200,212\nDL 251,36,70,0\nMvaa 92,200,252\nSA m0,s65,56224\nSA s70,s74,16384\n"
"Ta 194,246,776\nMvaa 84,200,248\nTa 196,246,4128\nMU 215,v243\nMvaa 120,204,248\nTa 198,246,4384\n"
"CV 212,212,213\nMvaa 128,204,252\nDL 251,36,71,0\nMvaa 136,204,212\nSA m0,s65,57280\nSA s71,s74,24576\n"
"Ta 200,246,4136\nMvaa 144,204,216\nTa 202,246,4392\nMU 234,v243\nMvaa 152,204,220\nTa 204,246,4640\n"
"MU 235,v243\nMvaa 156,208,220\nDL 251,36,70,0\nMvaa 148,208,216\nSA m0,s65,58336\nSA s70,s74,32768\n"
"Ta 206,246,4896\nMvaa 140,208,212\nSA s74,s60,s73\nCV 232,232,233\nCV 211,211,254\nMvaa 132,208,252\n"
"SA s73,s73,128\nv_rcp_f32 v244, v244\nMvaa 124,208,248\ns_waitcnt lgkmcnt(5)\nTa 208,246,4648\nCV 231,231,255\n"
"Mvaa 0,180,224\nDL 251,36,71,0\nMvaa 8,180,228\nSA m0,s65,59392\nSA s71,s75,65536\nTa 210,246,4904\n"
"Mvaa 16,180,232\ns_and_b32 s73, s73, 1023\nv_add_u32 v246, 0xfffe5900, v246\nCV 174,174,175\nMvaa 24,180,236\nCV 194,194,195\n"
"CV 213,214,215\nMvaa 32,180,240\nDL 251,36,70,0\nMvaa 36,184,240\nSA s70,s75,0\nCV 233,234,235\n"
"AR 176,32\nMvaa 28,184,236\nAR 177,33\nAR 196,72\nMvaa 20,184,232\nAR 197,73\n"
"AR 254,31\nMvaa 12,184,228\nAR 175,30\nAR 255,71\nMvaa 4,184,224\ns_waitcnt vmcnt(13) lgkmcnt(0)\n"
"AR 195,70\nAR 214,108\ns_barrier\nMvaa 40,188,224\nSA m0,s66,80256\nRa 248,245,0\n"
"AR 215,109\nMvaa 48,188,228\nDL 252,40,70,0\nMvaa 56,188,232\nSA m0,s66,81288\nSA s70,s75,131072\n"
"Ta 180,246,0\nMvaa 64,188,236\nTa 182,246,256\nAR 234,148\nMvaa 72,188,240\nRa 252,245,2112\n"
"AR 235,149\nMvaa 76,192,240\nDL 252,40,71,0\nMvaa 68,192,236\nSA m0,s66,82320\nSA s71,s75,196608\n"
"SA s75,s61,s93\nMvaa 60,192,232\nSA s93,s93,262144\ns_and_b32 s93, s93, 4194303\nRa 212,245,4224\nMvaa 52,192,228\n"
"Ra 216,245,6336\nMU 176,v244\nMvaa 44,192,224\nDL 252,40,70,0\nMvaa 80,196,224\nSA m0,s66,83352\n"
"SA s70,s62,s79\nRa 220,245,8448\nMvaa 88,196,228\nTa 184,246,8\nMU 177,v244\nMvaa 96,196,232\n"
"Ta 186,246,264\nAR 178,34\nMvaa 104,196,236\nDL 252,40,71,0\nMvaa 112,196,240\nSA m0,s67,0\n"
"SA s71,s75,65536\nTa 188,246,512\nMvaa 116,200,240\nSA s67,s67,s81\nTa 190,246,768\nAR 179,35\n"
"Mvaa 108,200,236\nTa 192,246,520\nMU 196,v244\nMvaa 100,200,232\nDN 250,44,70,0\nMvaa 92,200,228\n"
"Ta 194,246,776\nMU 197,v244\nMvaa 84,200,224\nTa 196,246,4128\nAR 198,74\nMvaa 120,204,224\n"
"Ta 198,246,4384\nAR 199,75\nMvaa 128,204,228\nDN 250,44,70,1024\nMvaa 136,204,232\nTa 200,246,4136\n"
"MU 254,v243\nMvaa 144,204,236\nTa 202,246,4392\nMU 175,v243\nMvaa 152,204,240\nTa 204,246,4640\n"
"MU 255,v243\nMvaa 156,208,240\nDN 250,44,70,2048\nMvaa 148,208,236\nTa 206,246,4896\nMU 195,v243\n"
"Mvaa 140,208,232\nMU 214,v243\nMU 215,v243\nMvaa 132,208,228\nMU 234,v243\nMU 235,v243\n"
"Mvaa 124,208,224\ns_waitcnt lgkmcnt(5)\nTa 208,246,4648\nMU 178,v244\nMvaa 0,180,248\nDN 250,44,70,3072\n"
"Mvaa 8,180,252\nSA s70,s75,0\nTa 210,246,4904\nMU 179,v244\nMvaa 16,180,212\nCV 176,176,177\n"
"MU 198,v244\nMvaa 24,180,216\nMU 199,v244\nCV 196,196,197\nMvaa 32,180,220\nCV 175,175,254\n"
"CV 195,195,255\nMvaa 36,184,220\nCV 214,214,215\nCV 234,234,235\nMvaa 28,184,216\nCV 177,178,179\n"
"CV 197,198,199\nMvaa 20,184,212\nAR 216,112\nAR 217,113\nMvaa 12,184,252\nAR 236,152\n"
"AR 237,153\nMvaa 4,184,248\ns_waitcnt vmcnt(13) lgkmcnt(0)\nAR 254,111\nAR 215,110\ns_barrier\n"
"Mvaa 40,188,248\nSA m0,s66,1024\nRa 224,245,512\nAR 255,151\nMvaa 48,188,252\nDL 252,40,70,0\n"
"Mvaa 56,188,212\nSA m0,s66,2056\nSA s70,s75,131072\nTa 180,246,37632\nMvaa 64,188,216\nTa 182,246,37888\n"
"AR 235,150\nMvaa 72,188,220\nRa 228,245,2624\nAR 178,36\nMvaa 76,192,220\nDL 252,40,71,0\n"
"Mvaa 68,192,216\nSA m0,s66,3088\nSA s71,s75,196608\nSA s75,s61,s93\nMvaa 60,192,212\nSA s93,s93,262144\n"
"s_and_b32 s93, s93, 4194303\nRa 232,245,4736\nMvaa 52,192,252\nRa 236,245,6848\nAR 179,37\nMvaa 44,192,248\n"
"DL 252,40,70,0\nMvaa 80,196,248\nSA m0,s66,4120\nSA s70,s74,0\nRa 240,245,8960\nMvaa 88,196,252\n"
"Ta 184,246,37640\nAR 198,76\nMvaa 96,196,212\nTa 186,246,37896\nAR 199,77\nMvaa 104,196,216\n"
"DL 252,40,71,0\nMvaa 112,196,220\nSA s71,s74,8192\nTa 188,246,38144\nMU 216,v244\nMvaa 116,200,220\n"
"Ta 190,246,38400\nMU 217,v244\nMvaa 108,200,216\nTa 192,246,38152\nAR 218,114\nMvaa 100,200,212\n"
"Ta 194,246,38408\nAR 219,115\nMvaa 92,200,252\nTa 196,246,41760\nMU 236,v244\nMvaa 84,200,248\n"
"Ta 198,246,42016\nMU 237,v244\nMvaa 120,204,248\nTa 200,246,41768\nAR 238,154\nMvaa 128,204,252\n"
"Ta 202,246,42024\nAR 239,155\nMvaa 136,204,212\nMU 254,v243\nMU 215,v243\nMvaa 144,204,216\n"
"MU 255,v243\nMU 235,v243\nMvaa 152,204,220\nTa 204,246,42272\nMU 178,v244\nMvaa 156,208,220\n"
"Ta 206,246,42528\nMU 179,v244\nMvaa 148,208,216\nMU 198,v244\nMU 199,v244\nMvaa 140,208,212\n"
"MU 218,v244\nMU 219,v244\nMvaa 132,208,252\nCV 216,216,217\nMU 238,v244\nMvaa 124,208,248\n"
"s_waitcnt lgkmcnt(4)\nTa 208,246,42280\nMU 239,v244\nMvaa 0,180,224\nTa 210,246,42536\nv_add_u32 v246, 0x13580, v246\n"
"Mvaa 8,180,228\nCV 236,236,237\nCV 215,215,254\nMvaa 16,180,232\nCV 235,235,255\nCV 178,178,179\n"
"Mvaa 24,180,236\nCV 198,198,199\nCV 217,218,219\nMvaa 32,180,240\nCV 237,238,239\nAR 254,39\n"
"Mvaa 36,184,240\nAR 179,38\nAR 255,79\nMvaa 28,184,236\nAR 199,78\nAR 218,116\n"
"Mvaa 20,184,232\nAR 219,117\nAR 238,156\nMvaa 12,184,228\nAR 239,157\nMU 254,v244\n"
"Mvaa 4,184,224\ns_waitcnt vmcnt(8) lgkmcnt(0)\nMU 179,v244\nMU 255,v244\ns_barrier\nMvaa 40,188,224\n"
"SA m0,s65,34048\nRa 248,245,37632\nMU 199,v244\nMvaa 48,188,228\nDL 251,36,70,0\nMvaa 56,188,232\n"
"SA m0,s65,35104\nSA s70,s74,16384\nTa 180,246,0\nMvaa 64,188,236\nTa 182,246,256\nMU 218,v244\n"
"Mvaa 72,188,240\nRa 252,245,39744\nMU 219,v244\nMvaa 76,192,240\nDL 251,36,71,0\nMvaa 68,192,236\n"
"SA m0,s65,36160\nSA s71,s74,24576\nRa 212,245,41856\nMvaa 60,192,232\nRa 216,245,43968\nMU 238,v244\n"
"Mvaa 52,192,228\nRa 220,245,46080\nMU 239,v244\nMvaa 44,192,224\nDL 251,36,70,0\nMvaa 80,196,224\n"
"SA m0,s65,37216\nSA s70,s74,32768\nTa 184,246,8\nMvaa 88,196,228\nTa 186,246,264\nCV 179,179,254\n"
"Mvaa 96,196,232\nTa 188,246,512\nCV 199,199,255\nMvaa 104,196,236\nDL 251,36,71,0\nMvaa 112,196,240\n"
"SA m0,s65,38272\nSA s71,s75,65536\nTa 190,246,768\nMvaa 116,200,240\nTa 192,246,520\nCV 218,218,219\n"
"Mvaa 108,200,236\nTa 194,246,776\nCV 238,238,239\nMvaa 100,200,232\nDL 251,36,70,0\nMvaa 92,200,228\n"
"SA s70,s75,0\nSA m0,s66,17536\nTa 196,246,4128\nMvaa 84,200,224\nTa 198,246,4384\nAR 254,119\n"
"Mvaa 120,204,224\nTa 200,246,4136\nAR 219,118\nMvaa 128,204,228\nDL 252,40,70,0\nMvaa 136,204,232\n"
"SA m0,s66,18568\nSA s70,s75,131072\nTa 202,246,4392\nMvaa 144,204,236\nAR 255,159\nAR 239,158\n"
"Mvaa 152,204,240\nTa 204,246,4640\nMU 254,v244\nMvaa 156,208,240\nDL 252,40,71,0\nMvaa 148,208,236\n"
"SA m0,s66,19600\nSA s71,s75,196608\nSA s75,s61,s93\nMvaa 140,208,232\nSA s93,s93,262144\nTa 206,246,4896\n"
"MU 219,v244\nMvaa 132,208,228\ns_and_b32 s93, s93, 4194303\nMU 255,v244\nMU 239,v244\nMvaa 124,208,224\n"
"s_waitcnt lgkmcnt(4)\nTa 208,246,4648\nCV 219,219,254\nMvaa 0,180,248\nDL 252,40,70,0\nMvaa 8,180,252\n"
"SA m0,s66,20632\nSA s70,s62,s79\nTa 210,246,4904\nMvaa 16,180,212\nSA s79,s79,8192\nCV 239,239,255\n"
"v_add_u32 v246, 0xfffeca80, v246\nMvaa 24,180,216\ns_and_b32 s79, s79, 0x1ffff\nv_lshrrev_b32 v254, 8, v253\nv_lshl_add_u32 v254, v254, 4, s82\nMvaa 32,180,220\n"
"DL 252,40,71,0\nMvaa 36,184,220\nSA m0,s68,0\nSA s71,s75,65536\nMvaa 28,184,216\nMvaa 20,184,212\n"
"Mvaa 12,184,252\nDN 250,48,70,0\nMvaa 4,184,248\ns_waitcnt vmcnt(10) lgkmcnt(0)\ns_barrier\nMvaa 40,188,248\n"
"Ra 224,245,38144\nMvaa 48,188,252\nDN 250,48,70,1024\nMvaa 56,188,212\nSA m0,s68,32\nTa 180,246,0\n"
"SA s68,s68,s81\nMvaa 64,188,216\nSA s80,s80,0x40000000\ns_cselect_b32 s81, s35, -33536\nTa 182,246,256\nMvaa 72,188,220\n"
"Ra 228,245,40256\nMvaa 76,192,220\nDN 250,48,70,2048\nMvaa 68,192,216\nRa 232,245,42368\nMvaa 60,192,212\n"
"Ra 236,245,44480\nMvaa 52,192,252\nRa 240,245,46592\nMvaa 44,192,248\nDN 250,48,70,3072\nMvaa 80,196,248\n"
"SA s70,s75,0\nTa 184,246,8\nMvaa 88,196,252\nTa 186,246,264\nMvaa 96,196,212\nTa 188,246,512\n"
"Mvaa 104,196,216\nTa 190,246,768\nMvaa 112,196,220\nTa 192,246,520\nMvaa 116,200,220\nTa 194,246,776\n"
"Mvaa 108,200,216\nTa 196,246,4128\nMvaa 100,200,212\nTa 198,246,4384\nMvaa 92,200,252\nMvaa 84,200,248\n"
"Ta 200,246,4136\nMvaa 120,204,248\nTa 202,246,4392\nMvaa 128,204,252\nMvaa 136,204,212\nMvaa 144,204,216\n"
"Mvaa 152,204,220\nTa 204,246,4640\nMvaa 156,208,220\nTa 206,246,4896\nMvaa 148,208,216\nMvaa 140,208,212\n"
"Mvaa 132,208,252\nMvaa 124,208,248\ns_waitcnt lgkmcnt(3)\nTa 208,246,4648\nMvaa 0,180,224\nTa 210,246,4904\n"
"Mvaa 8,180,228\nMvaa 16,180,232\nMvaa 24,180,236\nMvaa 32,180,240\nMvaa 36,184,240\nMvaa 28,184,236\n"
"Mvaa 20,184,232\nMvaa 12,184,228\nMvaa 4,184,224\ns_waitcnt vmcnt(4) lgkmcnt(0)\ns_barrier\nMvaa 40,188,224\n"
"SA m0,s66,1024\nRa 248,245,16512\nMvaa 48,188,228\nDL 252,40,70,0\nMvaa 56,188,232\nSA m0,s66,2056\n"
"SA s70,s75,131072\nTa 180,246,16512\nMvaa 64,188,236\nTa 182,246,16768\nMvaa 72,188,240\nRa 252,245,18624\n"
"Mvaa 76,192,240\nDL 252,40,71,0\nMvaa 68,192,236\nSA m0,s66,3088\nSA s71,s75,196608\nRa 212,245,20736\n"
"Mvaa 60,192,232\nRa 216,245,22848\nMvaa 52,192,228\nRa 220,245,24960\nMvaa 44,192,224\nDL 252,40,70,0\n"
"Mvaa 80,196,224\nSA m0,s66,4120\nTa 184,246,16520\nSA s70,s62,s79\nMvaa 88,196,228\nSA s79,s79,8192\n"
"Ta 186,246,16776\ns_and_b32 s79, s79, 0x1ffff\nMvaa 96,196,232\nTa 188,246,17024\nMvaa 104,196,236\nDL 252,40,71,0\n"
"Mvaa 112,196,240\nSA m0,s67,0\nTa 190,246,17280\nSA s67,s67,s81\nMvaa 116,200,240\nTa 192,246,17032\n"
"Mvaa 108,200,236\nTa 194,246,17288\nMvaa 100,200,232\nDN 250,44,70,0\nMvaa 92,200,228\nTa 196,246,20640\n"
"Mvaa 84,200,224\nTa 198,246,20896\nMvaa 120,204,224\nTa 200,246,20648\nMvaa 128,204,228\nTa 202,246,20904\n"
"Mvaa 136,204,232\nMvaa 144,204,236\nMvaa 152,204,240\nTa 204,246,21152\nMvaa 156,208,240\nTa 206,246,21408\n"
"Mvaa 148,208,236\nDN 250,44,70,1024\nMvaa 140,208,232\nMvaa 132,208,228\nMvaa 124,208,224\ns_waitcnt lgkmcnt(4)\n"
"Ta 208,246,21160\nMvaa 0,180,248\nTa 210,246,21416\nMvaa 8,180,252\nMvaa 16,180,212\nMvaa 24,180,216\n"
"Mvaa 32,180,220\nMvaa 36,184,220\nDN 250,44,70,2048\nMvaa 28,184,216\nMvaa 20,184,212\nMvaa 12,184,252\n"
"Mvaa 4,184,248\ns_waitcnt vmcnt(3) lgkmcnt(0)\ns_barrier\nMvaa 40,188,248\nRa 224,245,17024\nMvaa 48,188,252\n"
"Ta 180,246,0\nMvaa 56,188,212\nTa 182,246,256\nMvaa 64,188,216\nDN 250,44,70,3072\nMvaa 72,188,220\n"
"SA m0,s68,0\nRa 228,245,19136\nMvaa 76,192,220\nRa 232,245,21248\nMvaa 68,192,216\nRa 236,245,23360\n"
"Mvaa 60,192,212\nRa 240,245,25472\nMvaa 52,192,252\nTa 184,246,8\nMvaa 44,192,248\nTa 186,246,264\n"
"Mvaa 80,196,248\nTa 188,246,512\nMvaa 88,196,252\nTa 190,246,768\nMvaa 96,196,212\nDN 250,48,70,0\n"
"Mvaa 104,196,216\nTa 192,246,520\nMvaa 112,196,220\nTa 194,246,776\nMvaa 116,200,220\nTa 196,246,4128\n"
"Mvaa 108,200,216\nTa 198,246,4384\nMvaa 100,200,212\nMvaa 92,200,252\nMvaa 84,200,248\nTa 200,246,4136\n"
"Mvaa 120,204,248\nTa 202,246,4392\nMvaa 128,204,252\nDN 250,48,70,1024\nMvaa 136,204,212\nSA m0,s68,32\n"
"SA s68,s68,s81\nSA s80,s80,0x40000000\ns_cselect_b32 s81, s35, -33536\nMvaa 144,204,216\nMvaa 152,204,220\nTa 204,246,4640\n"
"Mvaa 156,208,220\nTa 206,246,4896\nMvaa 148,208,216\nMvaa 140,208,212\nMvaa 132,208,252\nMvaa 124,208,248\n"
"s_waitcnt lgkmcnt(3)\nTa 208,246,4648\nMvaa 0,180,224\nDN 250,48,70,2048\nMvaa 8,180,228\nTa 210,246,4904\n"
"Mvaa 16,180,232\nMvaa 24,180,236\nMvaa 32,180,240\nMvaa 4,184,224\nMvaa 40,188,224\nMU 0,s76\n"
"MU 1,s76\nMvaa 44,192,224\nMU 2,s76\nMU 3,s76\nMvaa 80,196,224\ns_waitcnt lgkmcnt(2)\n"
"CV 0,0,1\nMU 4,s76\nMvaa 84,200,224\ns_waitcnt lgkmcnt(0)\nMU 5,s76\nMU 6,s76\n"
"s_barrier\ns_cmp_lg_u32 s91, 1\ns_cbranch_scc1 1f\ns_sleep 6\n1:\nMvaa 120,204,224\n"
"DN 250,48,70,3072\nMvaa 124,208,224\nSA s70,s62,s79\nRa 0,254,0\nMU 7,s76\nMvaa 12,184,228\n"
"SA m0,s67,0\nRa 4,254,256\nCV 1,2,3\nMvaa 48,188,228\nSA s79,s79,8192\nRa 8,254,512\n"
"MU 40,s76\nMvaa 52,192,228\ns_and_b32 s79, s79, 0x1ffff\nRa 12,254,768\nMU 41,s76\nMvaa 88,196,228\n"
"SA s67,s67,s81\nCV 2,4,5\nCV 3,6,7\nMvaa 92,200,228\nMU 42,s76\nMU 43,s76\n"
"Mvaa 128,204,228\nCV 40,40,41\nMU 44,s76\nMvaa 132,208,228\nMU 45,s76\nMU 46,s76\n"
"Mvaa 20,184,232\nDN 250,44,70,0\nMvaa 56,188,232\nMU 47,s76\nCV 41,42,43\nMvaa 60,192,232\n"
"MU 80,s76\nMU 81,s76\nMvaa 96,196,232\nCV 42,44,45\nCV 43,46,47\nMvaa 100,200,232\n"
"MU 82,s76\nMU 83,s76\nMvaa 136,204,232\nCV 80,80,81\nMU 84,s76\nMvaa 140,208,232\n"
"MU 85,s76\nMU 86,s76\nMvaa 28,184,236\nMU 87,s76\nCV 81,82,83\nMvaa 64,188,236\n"
"MU 120,s76\nMU 121,s76\nMvaa 68,192,236\nDN 250,44,70,1024\nMvaa 104,196,236\nCV 82,84,85\n"
"CV 83,86,87\nMvaa 108,200,236\nMU 122,s76\nMU 123,s76\nMvaa 144,204,236\nCV 120,120,121\n"
"MU 124,s76\nMvaa 148,208,236\nMU 125,s76\nMU 126,s76\nMvaa 36,184,240\nMU 127,s76\n"
"CV 121,122,123\nMvaa 72,188,240\nMU 8,s76\nMU 9,s76\nMvaa 76,192,240\nCV 122,124,125\n"
"CV 123,126,127\nMvaa 112,196,240\nMU 10,s76\nMU 11,s76\nMvaa 116,200,240\nDN 250,44,70,2048\n"
"Mvaa 152,204,240\nCV 8,8,9\nMU 12,s76\nMvaa 156,208,240\ns_waitcnt lgkmcnt(0)\nMU 13,s76\n"
"MU 14,s76\nZvav 4,0,0\nMU 15,s76\nCV 9,10,11\nMvav 4,4,40\nMU 48,s76\n"
"MU 49,s76\nMvav 4,8,80\nCV 10,12,13\nCV 11,14,15\nMvav 4,12,120\nMU 50,s76\n"
"MU 51,s76\nCV 48,48,49\nMU 52,s76\nMU 53,s76\nMU 54,s76\nMU 55,s76\n"
"CV 49,50,51\nMU 88,s76\nMU 89,s76\nZvav 12,0,8\nRa 180,247,0\nCV 50,52,53\n"
"CV 51,54,55\nMU 90,s76\nMU 91,s76\nCV 88,88,89\nMU 92,s76\nMU 93,s76\n"
"MU 94,s76\nMU 95,s76\nCV 89,90,91\nMU 128,s76\nMU 129,s76\nMvav 12,4,48\n"
"Ra 196,247,1024\nCV 90,92,93\nCV 91,94,95\nMU 130,s76\nMU 131,s76\nCV 128,128,129\n"
"MU 132,s76\nMU 133,s76\nMU 134,s76\nMU 135,s76\nCV 129,130,131\nMU 16,s76\n"
"MU 17,s76\nMvav 12,8,88\nDN 250,44,70,3072\nRa 184,247,256\nCV 130,132,133\nCV 131,134,135\n"
"MU 18,s76\nMU 19,s76\nCV 16,16,17\nMU 20,s76\nMU 21,s76\nMU 22,s76\n"
"MU 23,s76\nCV 17,18,19\nMU 56,s76\nMU 57,s76\nMvav 12,12,128\nSA m0,s68,0\n"
"Ra 200,247,1280\nCV 18,20,21\nCV 19,22,23\nMU 58,s76\nMU 59,s76\nCV 56,56,57\n"
"MU 60,s76\nMU 61,s76\nMU 62,s76\nMU 63,s76\nCV 57,58,59\nMU 96,s76\n"
"MU 97,s76\nZvav 20,0,16\nRa 188,247,512\nCV 58,60,61\nCV 59,62,63\nMU 98,s76\n"
"MU 99,s76\nCV 96,96,97\nMU 100,s76\nMU 101,s76\nMU 102,s76\nMU 103,s76\n"
"CV 97,98,99\nMU 136,s76\nMU 137,s76\nMvav 20,4,56\nRa 204,247,1536\nCV 98,100,101\n"
"CV 99,102,103\nMU 138,s76\nMU 139,s76\nCV 136,136,137\nMU 140,s76\nMU 141,s76\n"
"MU 142,s76\nMU 143,s76\nCV 137,138,139\nMvav 20,8,96\nRa 192,247,768\nCV 138,140,141\n"
"CV 139,142,143\nRa 208,247,1792\nv_xor_b32 v4, 0x80000000, v4\nMvav 20,12,136\nv_xor_b32 v5, 0x80000000, v5\nv_xor_b32 v6, 0x80000000, v6\n"
"v_xor_b32 v7, 0x80000000, v7\ns_waitcnt lgkmcnt(7)\nMU 24,s76\nCvav 44,180,0,4\ns_waitcnt lgkmcnt(6)\nMU 25,s76\n"
"MU 26,s76\nCvav 84,196,0,4\ns_waitcnt lgkmcnt(5)\nMU 27,s76\nCV 24,24,25\nMvav 44,184,40\n"
"s_waitcnt lgkmcnt(4)\nMU 28,s76\nMU 29,s76\nMvav 84,200,40\ns_waitcnt lgkmcnt(3)\nMU 30,s76\n"
"MU 31,s76\nMvav 44,188,80\ns_waitcnt lgkmcnt(2)\nCV 25,26,27\nMU 64,s76\nMvav 84,204,80\n"
"s_waitcnt lgkmcnt(1)\nMU 65,s76\nCV 26,28,29\nMvav 44,192,120\ns_waitcnt lgkmcnt(0)\nCV 27,30,31\n"
"MU 66,s76\nMvav 84,208,120\nDN 250,48,70,0\nMU 67,s76\nCV 64,64,65\nMU 68,s76\n"
"MU 69,s76\nMU 70,s76\nMU 71,s76\nCV 65,66,67\nMU 104,s76\nMU 105,s76\n"
"Zvav 28,0,24\nCV 66,68,69\nCV 67,70,71\nMU 106,s76\nMU 107,s76\nCV 104,104,105\n"
"MU 108,s76\nMU 109,s76\nMU 110,s76\nMU 111,s76\nCV 105,106,107\nMU 144,s76\n"
"MU 145,s76\nMvav 28,4,64\nCV 106,108,109\nCV 107,110,111\nMU 146,s76\nMU 147,s76\n"
"CV 144,144,145\nMU 148,s76\nMU 149,s76\nMU 150,s76\nMU 151,s76\nCV 145,146,147\n"
"Mvav 28,8,104\nCV 146,148,149\nCV 147,150,151\nv_xor_b32 v12, 0x80000000, v12\nv_xor_b32 v13, 0x80000000, v13\nMvav 28,12,144\n"
"v_xor_b32 v14, 0x80000000, v14\nv_xor_b32 v15, 0x80000000, v15\nMU 32,s76\nMU 33,s76\nCvav 52,180,8,12\nMU 34,s76\n"
"MU 35,s76\nCvav 92,196,8,12\nCV 32,32,33\nMU 36,s76\nMvav 52,184,48\nMU 37,s76\n"
"MU 38,s76\nMvav 92,200,48\nMU 39,s76\nCV 33,34,35\nMvav 52,188,88\nDN 250,48,70,1024\n"
"Mvav 92,204,88\nSA m0,s68,32\nMU 72,s76\nMU 73,s76\nMvav 52,192,128\nSA s68,s68,s81\n"
"CV 34,36,37\nCV 35,38,39\nMvav 92,208,128\nMU 74,s76\nMU 75,s76\nCV 72,72,73\n"
"MU 76,s76\nMU 77,s76\nMU 78,s76\nMU 79,s76\nCV 73,74,75\nMU 112,s76\n"
"MU 113,s76\nZvav 36,0,32\nCV 74,76,77\nCV 75,78,79\nMU 114,s76\nMU 115,s76\n"
"CV 112,112,113\nMU 116,s76\nMU 117,s76\nMU 118,s76\nMU 119,s76\nCV 113,114,115\n"
"MU 152,s76\nMU 153,s76\nMvav 36,4,72\nCV 114,116,117\nCV 115,118,119\nMU 154,s76\n"
"MU 155,s76\nCV 152,152,153\nMU 156,s76\nMU 157,s76\nMU 158,s76\nMU 159,s76\n"
"CV 153,154,155\nMvav 36,8,112\nCV 154,156,157\nCV 155,158,159\nv_xor_b32 v20, 0x80000000, v20\nv_xor_b32 v21, 0x80000000, v21\n"
"Mvav 36,12,152\nv_xor_b32 v22, 0x80000000, v22\nv_xor_b32 v23, 0x80000000, v23\nTa 212,248,0\nTa 214,248,1024\nCvav 60,180,16,20\n"
"Ta 216,248,32\nv_xor_b32 v28, 0x80000000, v28\nCvav 100,196,16,20\nDN 250,48,70,2048\nTa 218,248,1056\nMvav 60,184,56\n"
"Ta 220,248,256\nv_xor_b32 v29, 0x80000000, v29\nTa 222,248,1280\nMvav 100,200,56\nTa 224,248,288\nv_xor_b32 v30, 0x80000000, v30\n"
"Ta 226,248,1312\nMvav 60,188,96\nTa 228,248,512\nv_xor_b32 v31, 0x80000000, v31\nTa 230,248,1536\nMvav 100,204,96\n"
"Ta 232,248,544\nv_xor_b32 v36, 0x80000000, v36\nTa 234,248,1568\nMvav 60,192,136\nTa 236,248,768\nv_xor_b32 v37, 0x80000000, v37\n"
"Ta 238,248,1792\nEX 44\nEX 45\nEX 46\nEX 47\nEX 84\n"
"EX 85\nEX 86\nEX 87\nMvav 100,208,136\nTa 240,248,800\nCV 124,44,45\n"
"Ta 242,248,1824\nCV 125,46,47\nCV 126,84,85\nCV 127,86,87\ns_waitcnt lgkmcnt(14)\nEX 52\n"
"Zaav 0,212,124\ns_waitcnt lgkmcnt(12)\nEX 53\nZaav 4,216,124\ns_waitcnt lgkmcnt(10)\nEX 54\n"
"Zaav 40,220,124\ns_waitcnt lgkmcnt(8)\nEX 55\nZaav 44,224,124\ns_waitcnt lgkmcnt(6)\nEX 92\n"
"Zaav 80,228,124\ns_waitcnt lgkmcnt(4)\nEX 93\nZaav 84,232,124\ns_waitcnt lgkmcnt(2)\nEX 94\n"
"Zaav 120,236,124\ns_waitcnt lgkmcnt(0)\nEX 95\nZaav 124,240,124\nDN 250,48,70,3072\nZaav 160,244,124\n"
"CV 132,52,53\nCV 133,54,55\ns_memrealtime s[94:95]\nCvav 68,180,24,28\nCV 134,92,93\nCV 135,94,95\n"
"Cvav 108,196,24,28\nv_xor_b32 v38, 0x80000000, v38\nv_xor_b32 v39, 0x80000000, v39\nMvav 68,184,64\nEX 60\nMvav 108,200,64\n"
"EX 61\nMvav 68,188,104\nEX 62\nMvav 108,204,104\nEX 63\nMvav 68,192,144\n"
"EX 100\nMvav 108,208,144\nEX 101\nPB0%=:\ns_waitcnt lgkmcnt(0)\ns_sub_u32 s92, s94, s72\n"
"s_cmp_lt_i32 s92, 0\ns_cbranch_scc1 PS0%=\nZaav 8,212,132\ns_cmp_lt_u32 s92, 10\ns_cselect_b32 s72, s72, s94\nSA s72,s72,s18\n"
"CV 140,60,61\nZaav 12,216,132\nEX 102\nZaav 48,220,132\nEX 103\nZaav 52,224,132\n"
"CV 141,62,63\nCV 142,100,101\nZaav 88,228,132\nCV 143,102,103\nv_add_u32 v247, s34, v247\nZaav 92,232,132\n"
"EX 68\nZaav 128,236,132\nEX 69\nZaav 132,240,132\nEX 70\nZaav 164,244,132\n"
"EX 71\ns_barrier\ns_cmp_lg_u32 s91, 1\ns_cbranch_scc1 1f\ns_sleep 6\n1:\n"
"Cvav 76,180,32,36\ns_and_b32 s8, s28, 0xffff\nRa 180,247,0\nCV 148,68,69\nCvav 116,196,32,36\ns_bfe_i32 s15, s8, 0x10000\n"
"Ra 196,247,1024\nCV 149,70,71\nMvav 76,184,72\nSA s70,s62,s79\nRa 184,247,256\nv_add_u32 v248, s34, v248\n"
"Mvav 116,200,72\ns_andn2_b32 s26, 0x2000, s15\nRa 200,247,1280\nSA s79,s79,s26\nMvav 76,188,112\ns_lshl_b32 s26, s26, 17\n"
"Ra 188,247,512\nSA s80,s80,s26\ns_cselect_b32 s81, s35, -33536\nMvav 116,204,112\ns_bitcmp1_b32 s8, 0\ns_cselect_b32 s70, 0x7ff00000, s70\n"
"Ra 204,247,1536\ns_bitcmp1_b32 s8, 0\ns_cselect_b32 s9, s30, s67\nMvav 76,192,152\ns_bitcmp1_b32 s8, 0\ns_cselect_b32 s14, s30, s68\n"
"Ra 192,247,768\ns_andn2_b32 s81, s81, s15\nMvav 116,208,152\ns_lshr_b32 s8, s8, 1\nRa 208,247,1792\ns_and_b32 s79, s79, 0x1ffff\n"
"Zaav 16,212,140\nSA s67,s67,s81\nEX 108\nZaav 20,216,140\nSA s68,s68,s81\nEX 109\n"
"Zaav 56,220,140\nSA m0,s9,0\nEX 110\nZaav 60,224,140\nDL 250,44,70,0\nZaav 96,228,140\n"
"SA s33,s33,0x40000000\ns_cselect_b32 s34, s35, -33536\nEX 111\nZaav 100,232,140\nCV 150,108,109\nCV 151,110,111\n"
"Zaav 136,236,140\nEX 76\nZaav 140,240,140\nEX 77\nZaav 168,244,140\ns_waitcnt lgkmcnt(1)\n"
"EX 78\nCvav 44,180,0,4\nEX 79\nCvav 84,196,0,4\nEX 116\nMvav 44,184,40\n"
"EX 117\nMvav 84,200,40\nDL 250,44,70,1024\nMvav 44,188,80\nEX 118\nMvav 84,204,80\n"
"EX 119\nMvav 44,192,120\ns_waitcnt lgkmcnt(0)\nCV 156,76,77\nCV 157,78,79\nMvav 84,208,120\n"
"CV 158,116,117\nCV 159,118,119\nZaav 24,212,148\nv_add_u32 v247, s34, v247\nZaav 28,216,148\nEX 44\n"
"Zaav 64,220,148\nEX 45\nZaav 68,224,148\nEX 46\nZaav 104,228,148\nDL 250,44,70,2048\n"
"Zaav 108,232,148\nEX 47\nZaav 144,236,148\nEX 84\nZaav 148,240,148\nEX 85\n"
"Zaav 172,244,148\nEX 86\nCvav 52,180,8,12\nEX 87\nCvav 92,196,8,12\nCV 124,44,45\n"
"CV 125,46,47\nMvav 52,184,48\nCV 126,84,85\nCV 127,86,87\nMvav 92,200,48\nMvav 52,188,88\n"
"DL 250,44,70,3072\nMvav 92,204,88\nSA m0,s14,0\nMvav 52,192,128\nMvav 92,208,128\nZaav 32,212,156\n"
"Ta 212,248,0\nZaav 36,216,156\nTa 214,248,1024\nZaav 72,220,156\nTa 216,248,32\nZaav 76,224,156\n"
"Ta 218,248,1056\nZaav 112,228,156\nTa 220,248,256\nZaav 116,232,156\nDN 250,48,70,0\nZaav 152,236,156\n"
"Ta 222,248,1280\nZaav 156,240,156\nTa 224,248,288\nZaav 176,244,156\nTa 226,248,1312\nCvav 60,180,16,20\n"
"Ta 228,248,512\nCvav 100,196,16,20\nTa 230,248,1536\nMvav 60,184,56\nTa 232,248,544\nMvav 100,200,56\n"
"Ta 234,248,1568\nMvav 60,188,96\nTa 236,248,768\nMvav 100,204,96\nDN 250,48,70,1024\nMvav 60,192,136\n"
"SA m0,s14,32\nTa 238,248,1792\nMvav 100,208,136\ns_waitcnt lgkmcnt(8)\nTa 240,248,800\nTa 242,248,1824\n"
"Maav 0,212,124\nEX 52\nMaav 4,216,124\nEX 53\nMaav 40,220,124\ns_waitcnt lgkmcnt(7)\n"
"EX 54\nMaav 44,224,124\ns_waitcnt lgkmcnt(6)\nEX 55\nMaav 80,228,124\ns_waitcnt lgkmcnt(4)\n"
"EX 92\nMaav 84,232,124\ns_waitcnt lgkmcnt(2)\nEX 93\nMaav 120,236,124\ns_waitcnt lgkmcnt(0)\n"
"EX 94\nMaav 124,240,124\nDN 250,48,70,2048\nMaav 160,244,124\nEX 95\ns_memrealtime s[94:95]\n"
"Cvav 68,180,24,28\nCV 132,52,53\nCV 133,54,55\nCvav 108,196,24,28\nCV 134,92,93\nCV 135,94,95\n"
"Mvav 68,184,64\nEX 60\nMvav 108,200,64\nEX 61\nMvav 68,188,104\nEX 62\n"
"Mvav 108,204,104\nEX 63\nMvav 68,192,144\nDN 250,48,70,3072\nMvav 108,208,144\nEX 100\n"
"PB1%=:\ns_waitcnt lgkmcnt(0)\ns_sub_u32 s92, s94, s72\ns_cmp_lt_i32 s92, 0\ns_cbranch_scc1 PS1%=\nMaav 8,212,132\n"
"s_cmp_lt_u32 s92, 10\ns_cselect_b32 s72, s72, s94\nSA s72,s72,s18\nCV 140,60,61\nMaav 12,216,132\nEX 101\n"
"Maav 48,220,132\nEX 102\nMaav 52,224,132\nEX 103\nMaav 88,228,132\nCV 141,62,63\n"
"CV 142,100,101\nMaav 92,232,132\nCV 143,102,103\nv_add_u32 v248, s34, v248\nMaav 128,236,132\nEX 68\n"
"Maav 132,240,132\nEX 69\nMaav 164,244,132\ns_waitcnt vmcnt(16)\nEX 70\ns_barrier\n"
"Cvav 76,180,32,36\nSA s70,s62,s79\nRa 180,247,0\nCV 148,68,69\nCvav 116,196,32,36\ns_bitcmp1_b32 s8, 0\n"
"s_cselect_b32 s9, s30, s67\nRa 196,247,1024\ns_bitcmp1_b32 s8, 0\ns_cselect_b32 s70, 0x7ff00000, s70\nMvav 76,184,72\nSA m0,s9,0\n"
"Ra 184,247,256\ns_bitcmp1_b32 s8, 0\ns_cselect_b32 s14, s30, s68\nMvav 116,200,72\nDL 250,44,70,0\nMvav 76,188,112\n"
"s_bfe_i32 s15, s8, 0x10000\nRa 200,247,1280\ns_andn2_b32 s26, 0x2000, s15\nMvav 116,204,112\nSA s79,s79,s26\nRa 188,247,512\n"
"s_lshl_b32 s26, s26, 17\nMvav 76,192,152\nSA s80,s80,s26\ns_cselect_b32 s81, s35, -33536\nRa 204,247,1536\ns_andn2_b32 s81, s81, s15\n"
"Mvav 116,208,152\ns_lshr_b32 s8, s8, 1\nRa 192,247,768\ns_and_b32 s79, s79, 0x1ffff\nMaav 16,212,140\nSA s67,s67,s81\n"
"Ra 208,247,1792\nSA s68,s68,s81\nMaav 20,216,140\nSA s33,s33,0x40000000\ns_cselect_b32 s34, s35, -33536\nEX 71\n"
"Maav 56,220,140\nEX 108\nMaav 60,224,140\nEX 109\nMaav 96,228,140\nDL 250,44,70,1024\n"
"Maav 100,232,140\nEX 110\nMaav 136,236,140\nEX 111\nMaav 140,240,140\nCV 149,70,71\n"
"CV 150,108,109\nMaav 168,244,140\ns_waitcnt lgkmcnt(2)\nCV 151,110,111\nv_add_u32 v247, s34, v247\nCvav 44,180,0,4\n"
"EX 76\nCvav 84,196,0,4\nEX 77\nMvav 44,184,40\nEX 78\nMvav 84,200,40\n"
"EX 79\nMvav 44,188,80\nDL 250,44,70,2048\nMvav 84,204,80\ns_waitcnt lgkmcnt(0)\nEX 116\n"
"Mvav 44,192,120\nEX 117\nMvav 84,208,120\nEX 118\nMaav 24,212,148\nEX 119\n"
"Maav 28,216,148\nCV 156,76,77\nCV 157,78,79\nMaav 64,220,148\nCV 158,116,117\nCV 159,118,119\n"
"Maav 68,224,148\nEX 44\nMaav 104,228,148\nEX 45\nMaav 108,232,148\nDL 250,44,70,3072\n"
"Maav 144,236,148\nSA m0,s14,0\nEX 46\nMaav 148,240,148\nEX 47\nMaav 172,244,148\n"
"EX 84\nCvav 52,180,8,12\nEX 85\nCvav 92,196,8,12\nEX 86\nMvav 52,184,48\n"
"EX 87\nMvav 92,200,48\nCV 124,44,45\nCV 125,46,47\nMvav 52,188,88\nCV 126,84,85\n"
"CV 127,86,87\nMvav 92,204,88\nDN 250,48,70,0\nMvav 52,192,128\nMvav 92,208,128\nMaav 32,212,156\n"
"Ta 212,248,0\nMaav 36,216,156\nTa 214,248,1024\nMaav 72,220,156\nTa 216,248,32\nMaav 76,224,156\n"
"Ta 218,248,1056\nMaav 112,228,156\nTa 220,248,256\nMaav 116,232,156\nTa 222,248,1280\nMaav 152,236,156\n"
"DN 250,48,70,1024\nMaav 156,240,156\nSA m0,s14,32\nTa 224,248,288\nMaav 176,244,156\nTa 226,248,1312\n"
"Cvav 60,180,16,20\nTa 228,248,512\nCvav 100,196,16,20\nTa 230,248,1536\nMvav 60,184,56\nTa 232,248,544\n"
"Mvav 100,200,56\nTa 234,248,1568\nMvav 60,188,96\nTa 236,248,768\nMvav 100,204,96\nTa 238,248,1792\n"
"Mvav 60,192,136\nDN 250,48,70,2048\nMvav 100,208,136\ns_waitcnt lgkmcnt(8)\nTa 240,248,800\nTa 242,248,1824\n"
"Maav 0,212,124\nEX 52\nMaav 4,216,124\nEX 53\nMaav 40,220,124\ns_waitcnt lgkmcnt(7)\n"
"EX 54\nMaav 44,224,124\ns_waitcnt lgkmcnt(6)\nEX 55\nMaav 80,228,124\ns_waitcnt lgkmcnt(4)\n"
"EX 92\nMaav 84,232,124\ns_waitcnt lgkmcnt(2)\nEX 93\nMaav 120,236,124\ns_waitcnt lgkmcnt(0)\n"
"EX 94\nMaav 124,240,124\nDN 250,48,70,3072\nMaav 160,244,124\nEX 95\ns_memrealtime s[94:95]\n"
"Cvav 68,180,24,28\nCV 132,52,53\nCV 133,54,55\nCvav 108,196,24,28\nCV 134,92,93\nCV 135,94,95\n"
"Mvav 68,184,64\nEX 60\nMvav 108,200,64\nEX 61\nMvav 68,188,104\nEX 62\n"
"Mvav 108,204,104\nEX 63\nMvav 68,192,144\nEX 100\nMvav 108,208,144\nEX 101\n"
"PB2%=:\ns_waitcnt lgkmcnt(0)\ns_sub_u32 s92, s94, s72\ns_cmp_lt_i32 s92, 0\ns_cbranch_scc1 PS2%=\nMaav 8,212,132\n"
"s_cmp_lt_u32 s92, 10\ns_cselect_b32 s72, s72, s94\nSA s72,s72,s18\nCV 140,60,61\nMaav 12,216,132\nEX 102\n"
"Maav 48,220,132\nEX 103\nMaav 52,224,132\nCV 141,62,63\nCV 142,100,101\nMaav 88,228,132\n"
"CV 143,102,103\nv_add_u32 v248, s34, v248\nMaav 92,232,132\nEX 68\nMaav 128,236,132\nEX 69\n"
"Maav 132,240,132\nEX 70\nMaav 164,244,132\ns_waitcnt vmcnt(16)\nEX 71\ns_barrier\n"
"ALP%=:\nCvav 76,180,32,36\nSA s70,s62,s79\nRa 180,247,0\nCV 148,68,69\nCvav 116,196,32,36\n"
"s_bitcmp1_b32 s8, 0\ns_cselect_b32 s9, s30, s67\nRa 196,247,1024\nCV 149,70,71\nMvav 76,184,72\ns_bitcmp1_b32 s8, 0\n"
"s_cselect_b32 s70, 0x7ff00000, s70\nRa 184,247,256\nSA m0,s9,0\nMvav 116,200,72\nDL 250,44,70,0\nMvav 76,188,112\n"
"s_bitcmp1_b32 s8, 0\ns_cselect_b32 s14, s30, s68\nRa 200,247,1280\ns_bfe_i32 s15, s8, 0x10000\nMvav 116,204,112\ns_andn2_b32 s26, 0x2000, s15\n"
"Ra 188,247,512\nSA s79,s79,s26\nMvav 76,192,152\ns_lshl_b32 s26, s26, 17\nRa 204,247,1536\nSA s80,s80,s26\n"
"s_cselect_b32 s81, s35, -33536\nMvav 116,208,152\ns_andn2_b32 s81, s81, s15\nRa 192,247,768\ns_lshr_b32 s8, s8, 1\nMaav 16,212,140\n"
"s_and_b32 s79, s79, 0x1ffff\nRa 208,247,1792\nSA s67,s67,s81\nMaav 20,216,140\nSA s68,s68,s81\nEX 108\n"
"Maav 56,220,140\nSA s33,s33,0x40000000\ns_cselect_b32 s34, s35, -33536\nEX 109\nMaav 60,224,140\nEX 110\n"
"Maav 96,228,140\nDL 250,44,70,1024\nMaav 100,232,140\nEX 111\nMaav 136,236,140\nCV 150,108,109\n"
"CV 151,110,111\nMaav 140,240,140\nEX 76\nMaav 168,244,140\ns_waitcnt lgkmcnt(2)\nEX 77\n"
"Cvav 44,180,0,4\nEX 78\nCvav 84,196,0,4\nEX 79\nMvav 44,184,40\nEX 116\n"
"Mvav 84,200,40\nEX 117\nMvav 44,188,80\nDL 250,44,70,2048\nMvav 84,204,80\ns_waitcnt lgkmcnt(0)\n"
"EX 118\nMvav 44,192,120\nEX 119\nMvav 84,208,120\nCV 156,76,77\nCV 157,78,79\n"
"Maav 24,212,148\nCV 158,116,117\nCV 159,118,119\nMaav 28,216,148\nEX 44\nMaav 64,220,148\n"
"EX 45\nMaav 68,224,148\nEX 46\nMaav 104,228,148\nEX 47\nMaav 108,232,148\n"
"DL 250,44,70,3072\nMaav 144,236,148\nSA m0,s14,0\nEX 84\nMaav 148,240,148\nEX 85\n"
"Maav 172,244,148\nEX 86\nCvav 52,180,8,12\nEX 87\nCvav 92,196,8,12\nCV 124,44,45\n"
"CV 125,46,47\nMvav 52,184,48\nCV 126,84,85\nCV 127,86,87\nMvav 92,200,48\nv_add_u32 v247, s34, v247\n"
"Mvav 52,188,88\nMvav 92,204,88\nDN 250,48,70,0\nMvav 52,192,128\nMvav 92,208,128\nMaav 32,212,156\n"
"Ta 212,248,0\nMaav 36,216,156\nTa 214,248,1024\nMaav 72,220,156\nTa 216,248,32\nMaav 76,224,156\n"
"Ta 218,248,1056\nMaav 112,228,156\nTa 220,248,256\nMaav 116,232,156\nTa 222,248,1280\nMaav 152,236,156\n"
"DN 250,48,70,1024\nMaav 156,240,156\nSA m0,s14,32\nTa 224,248,288\nMaav 176,244,156\nTa 226,248,1312\n"
"Cvav 60,180,16,20\nTa 228,248,512\nCvav 100,196,16,20\nTa 230,248,1536\nMvav 60,184,56\nTa 232,248,544\n"
"Mvav 100,200,56\nTa 234,248,1568\nMvav 60,188,96\nTa 236,248,768\nMvav 100,204,96\nTa 238,248,1792\n"
"Mvav 60,192,136\nDN 250,48,70,2048\nMvav 100,208,136\ns_waitcnt lgkmcnt(8)\nTa 240,248,800\nTa 242,248,1824\n"
"Maav 0,212,124\nEX 52\nMaav 4,216,124\nEX 53\nMaav 40,220,124\ns_waitcnt lgkmcnt(7)\n"
"EX 54\nMaav 44,224,124\ns_waitcnt lgkmcnt(6)\nEX 55\nMaav 80,228,124\ns_waitcnt lgkmcnt(4)\n"
"EX 92\nMaav 84,232,124\ns_waitcnt lgkmcnt(2)\nEX 93\nMaav 120,236,124\ns_waitcnt lgkmcnt(0)\n"
"EX 94\nMaav 124,240,124\nDN 250,48,70,3072\nMaav 160,244,124\nEX 95\ns_memrealtime s[94:95]\n"
"Cvav 68,180,24,28\nCV 132,52,53\nCV 133,54,55\nCvav 108,196,24,28\nCV 134,92,93\nCV 135,94,95\n"
"Mvav 68,184,64\nEX 60\nMvav 108,200,64\nEX 61\nMvav 68,188,104\nEX 62\n"
"Mvav 108,204,104\nEX 63\nMvav 68,192,144\nEX 100\nMvav 108,208,144\nEX 101\n"
"PB3%=:\ns_waitcnt lgkmcnt(0)\ns_sub_u32 s92, s94, s72\ns_cmp_lt_i32 s92, 0\ns_cbranch_scc1 PS3%=\nMaav 8,212,132\n"
"s_cmp_lt_u32 s92, 10\ns_cselect_b32 s72, s72, s94\nSA s72,s72,s18\nCV 140,60,61\nMaav 12,216,132\nEX 102\n"
"Maav 48,220,132\nEX 103\nMaav 52,224,132\nCV 141,62,63\nCV 142,100,101\nMaav 88,228,132\n"
"CV 143,102,103\nv_add_u32 v248, s34, v248\nMaav 92,232,132\nEX 68\nMaav 128,236,132\nEX 69\n"
"Maav 132,240,132\nEX 70\nMaav 164,244,132\ns_waitcnt vmcnt(8)\nEX 71\ns_barrier\n"
"s_sub_u32 s90, s90, 1\ns_cbranch_scc0 ALP%=\ns_bitcmp1_b32 s91, 0\ns_cbranch_scc1 TL2%=\nCvav 76,180,32,36\nSA s74,s60,s73\n"
"Ra 180,247,0\nCV 148,68,69\nCvav 116,196,32,36\nSA s70,s74,0\nRa 196,247,1024\nCV 149,70,71\n"
"Mvav 76,184,72\nSA m0,s65,1024\nRa 184,247,256\nv_lshlrev_b32 v160, 5, v253\nMvav 116,200,72\nDL 251,36,70,0\n"
"Mvav 76,188,112\nSA s71,s74,8192\nRa 200,247,1280\nv_cmp_gt_u32 vcc, 0x70, v253\nMvav 116,204,112\nSA m0,s65,2080\n"
"Ra 188,247,512\nv_mov_b32 v161, 104857600\nMvav 76,192,152\nSA s70,s74,16384\nRa 204,247,1536\nv_cndmask_b32 v160, v161, v160, vcc\n"
"Mvav 116,208,152\nDL 251,36,71,0\nMaav 16,212,140\nSA m0,s65,3136\nRa 192,247,768\nv_add_u32 v160, s87, v160\n"
"Maav 20,216,140\nSA s71,s74,24576\nRa 208,247,1792\nv_add_u32 v160, s96, v160\nMaav 56,220,140\nSA s61,s61,256\n"
"EX 108\nMaav 60,224,140\nbuffer_load_dword v161, v160, s[56:59], 0 offen sc1\nMaav 96,228,140\nSA s75,s61,s93\nEX 109\n"
"Maav 100,232,140\nSA s93,s93,262144\nEX 110\nMaav 136,236,140\ns_and_b32 s93, s93, 4194303\nEX 111\n"
"Maav 140,240,140\nDL 251,36,70,0\nMaav 168,244,140\ns_waitcnt lgkmcnt(2)\nSA m0,s65,4192\nSA s70,s74,32768\n"
"Cvav 44,180,0,4\nSA s73,s73,128\nCV 150,108,109\nCV 151,110,111\nCvav 84,196,0,4\ns_and_b32 s73, s73, 1023\n"
"EX 76\nMvav 44,184,40\nDL 251,36,71,0\nMvav 84,200,40\nSA m0,s65,5248\nSA s71,s75,65536\n"
"SA s33,s33,0x40000000\ns_cselect_b32 s34, s35, -33536\nMvav 44,188,80\nSA s62,s62,131072\nEX 77\nMvav 84,204,80\n"
"s_waitcnt lgkmcnt(0)\nEX 78\nMvav 44,192,120\nDL 251,36,70,0\nMvav 84,208,120\nSA s70,s75,0\n"
"SA m0,s66,109312\nCV 156,76,77\nMaav 24,212,148\nEX 79\nMaav 28,216,148\nEX 116\n"
"Maav 64,220,148\nDL 252,40,70,0\nMaav 68,224,148\nSA m0,s66,110344\nSA s70,s75,131072\nCV 157,78,79\n"
"Maav 104,228,148\nEX 117\nMaav 108,232,148\nEX 118\nMaav 144,236,148\nDL 252,40,71,0\n"
"Maav 148,240,148\nSA m0,s66,111376\nSA s71,s75,196608\nCV 158,116,117\nMaav 172,244,148\nEX 119\n"
"Cvav 52,180,8,12\nCV 159,118,119\nv_add_u32 v247, s34, v247\nCvav 92,196,8,12\nDL 252,40,70,0\nMvav 52,184,48\n"
"SA m0,s66,112408\nEX 44\nMvav 92,200,48\nEX 45\nMvav 52,188,88\nEX 46\n"
"Mvav 92,204,88\nDL 252,40,71,0\nMvav 52,192,128\nEX 47\nMvav 92,208,128\nEX 84\n"
"Maav 32,212,156\nTa 212,248,0\nCV 124,44,45\nMaav 36,216,156\nTa 214,248,1024\nCV 125,46,47\n"
"Maav 72,220,156\nTa 216,248,32\nMaav 76,224,156\nTa 218,248,1056\nMaav 112,228,156\nTa 220,248,256\n"
"Maav 116,232,156\nTa 222,248,1280\nMaav 152,236,156\nTa 224,248,288\nMaav 156,240,156\nTa 226,248,1312\n"
"Maav 176,244,156\nTa 228,248,512\nCvav 60,180,16,20\nTa 230,248,1536\nCvav 100,196,16,20\nTa 232,248,544\n"
"Mvav 60,184,56\nTa 234,248,1568\nMvav 100,200,56\nEX 85\nMvav 60,188,96\nEX 86\n"
"Mvav 100,204,96\nEX 87\nMvav 60,192,136\nTa 236,248,768\nCV 126,84,85\nTa 238,248,1792\n"
"Mvav 100,208,136\nCV 127,86,87\nTa 240,248,800\nTa 242,248,1824\ns_waitcnt lgkmcnt(14)\nMaav 0,212,124\n"
"s_waitcnt lgkmcnt(8)\nEX 52\nMaav 4,216,124\nEX 53\nMaav 40,220,124\nEX 54\n"
"Maav 44,224,124\ns_waitcnt lgkmcnt(5)\nEX 55\nMaav 80,228,124\ns_waitcnt lgkmcnt(4)\nEX 92\n"
"Maav 84,232,124\ns_waitcnt lgkmcnt(2)\nEX 93\nMaav 120,236,124\ns_waitcnt lgkmcnt(0)\nEX 94\n"
"Maav 124,240,124\nEX 95\nMaav 160,244,124\nCV 132,52,53\nCV 133,54,55\ns_memrealtime s[94:95]\n"
"Cvav 68,180,24,28\nCV 134,92,93\nCV 135,94,95\nCvav 108,196,24,28\nEX 60\nMvav 68,184,64\n"
"EX 61\nMvav 108,200,64\nEX 62\nMvav 68,188,104\nEX 63\nMvav 108,204,104\n"
"EX 100\nMvav 68,192,144\nEX 101\nMvav 108,208,144\nEX 102\nPB4%=:\n"
"s_waitcnt lgkmcnt(0)\ns_sub_u32 s92, s94, s72\ns_cmp_lt_i32 s92, 0\ns_cbranch_scc1 PS4%=\nMaav 8,212,132\ns_cmp_lt_u32 s92, 10\n"
"s_cselect_b32 s72, s72, s94\nSA s72,s72,s18\nCV 140,60,61\nMaav 12,216,132\nEX 103\nMaav 48,220,132\n"
"CV 141,62,63\nCV 142,100,101\nMaav 52,224,132\nCV 143,102,103\nv_add_u32 v248, s34, v248\nMaav 88,228,132\n"
"EX 68\nMaav 92,232,132\nEX 69\nMaav 128,236,132\nEX 70\nMaav 132,240,132\n"
"EX 71\nMaav 164,244,132\ns_waitcnt vmcnt(10)\nEX 108\ns_barrier\nCvav 76,180,32,36\n"
"SA s75,s61,s93\nSA s70,s75,0\nSA m0,s66,67072\nCvav 116,196,32,36\nDL 252,40,70,0\nMvav 76,184,72\n"
"SA s71,s75,65536\nSA m0,s66,68104\nSA s70,s75,131072\nMvav 116,200,72\nSA s74,s60,s73\nRa 180,247,0\n"
"CV 148,68,69\nMvav 76,188,112\nSA s93,s93,262144\nRa 196,247,1024\nCV 149,70,71\nMvav 116,204,112\n"
"DL 252,40,71,0\nMvav 76,192,152\nSA m0,s66,69136\nSA s71,s75,196608\nRa 184,247,256\nMvav 116,208,152\n"
"s_and_b32 s93, s93, 4194303\nRa 200,247,1280\nv_add_u32 v246, 0x1a700, v246\nMaav 16,212,140\nSA s75,s61,s93\nRa 188,247,512\n"
"v_add_u32 v245, 0xffffbf80, v245\nMaav 20,216,140\nDL 252,40,70,0\nMaav 56,220,140\nSA m0,s66,70168\nSA s70,s74,0\n"
"Ra 204,247,1536\nMaav 60,224,140\nSA s93,s93,262144\nRa 192,247,768\ns_and_b32 s93, s93, 4194303\nMaav 96,228,140\n"
"SA s73,s73,128\nRa 208,247,1792\ns_and_b32 s73, s73, 1023\nMaav 100,232,140\nDL 252,40,71,0\nMaav 136,236,140\n"
"SA m0,s65,88192\nSA s71,s74,8192\nSA s33,s33,0x40000000\ns_cselect_b32 s34, s35, -33536\nMaav 140,240,140\nEX 109\n"
"Maav 168,244,140\ns_waitcnt lgkmcnt(5)\nEX 110\nCvav 44,180,0,4\nDL 251,36,70,0\nCvav 84,196,0,4\n"
"SA m0,s65,89248\nSA s70,s74,16384\nCV 150,108,109\nMvav 44,184,40\ns_waitcnt lgkmcnt(3)\nEX 111\n"
"Mvav 84,200,40\nCV 151,110,111\nv_add_u32 v247, s34, v247\nMvav 44,188,80\ns_waitcnt lgkmcnt(1)\nEX 76\n"
"Mvav 84,204,80\nDL 251,36,71,0\nMvav 44,192,120\ns_waitcnt lgkmcnt(0)\nSA m0,s65,90304\nSA s71,s74,24576\n"
"Mvav 84,208,120\nEX 77\nMaav 24,212,148\nEX 78\nMaav 28,216,148\nDL 251,36,70,0\n"
"Maav 64,220,148\nSA m0,s65,91360\nSA s70,s74,32768\nCV 156,76,77\nMaav 68,224,148\nEX 79\n"
"Maav 104,228,148\nEX 116\nMaav 108,232,148\nDL 251,36,71,0\nMaav 144,236,148\nSA m0,s65,92416\n"
"SA s71,s75,65536\nCV 157,78,79\nMaav 148,240,148\nEX 117\nMaav 172,244,148\nEX 118\n"
"Cvav 52,180,8,12\nDL 251,36,70,0\nCvav 92,196,8,12\nSA s70,s75,0\nEX 119\nMvav 52,184,48\n"
"CV 158,116,117\nCV 159,118,119\nMvav 92,200,48\nEX 44\nMvav 52,188,88\nEX 45\n"
"Mvav 92,204,88\nEX 46\nMvav 52,192,128\nEX 47\nMvav 92,208,128\nEX 84\n"
"Maav 32,212,156\nTa 212,248,0\nCV 124,44,45\nMaav 36,216,156\nTa 214,248,1024\nCV 125,46,47\n"
"Maav 72,220,156\nTa 216,248,32\nMaav 76,224,156\nTa 218,248,1056\nMaav 112,228,156\nTa 220,248,256\n"
"Maav 116,232,156\nTa 222,248,1280\nMaav 152,236,156\nTa 224,248,288\nMaav 156,240,156\nTa 226,248,1312\n"
"Maav 176,244,156\nTa 228,248,512\nCvav 60,180,16,20\nTa 230,248,1536\nCvav 100,196,16,20\nTa 232,248,544\n"
"Mvav 60,184,56\nTa 234,248,1568\nMvav 100,200,56\nEX 85\nMvav 60,188,96\nEX 86\n"
"Mvav 100,204,96\nEX 87\nMvav 60,192,136\nTa 236,248,768\nCV 126,84,85\nTa 238,248,1792\n"
"Mvav 100,208,136\nCV 127,86,87\nTa 240,248,800\nTa 242,248,1824\ns_waitcnt lgkmcnt(14)\nMaav 0,212,124\n"
"s_waitcnt lgkmcnt(8)\nEX 52\nMaav 4,216,124\nEX 53\nMaav 40,220,124\nEX 54\n"
"Maav 44,224,124\ns_waitcnt lgkmcnt(5)\nEX 55\nMaav 80,228,124\ns_waitcnt lgkmcnt(4)\nEX 92\n"
"Maav 84,232,124\ns_waitcnt lgkmcnt(2)\nEX 93\nMaav 120,236,124\ns_waitcnt lgkmcnt(0)\nEX 94\n"
"Maav 124,240,124\nEX 95\nMaav 160,244,124\nCV 132,52,53\nCV 133,54,55\nCvav 68,180,24,28\n"
"CV 134,92,93\nCV 135,94,95\nCvav 108,196,24,28\nEX 60\nMvav 68,184,64\nEX 61\n"
"Mvav 108,200,64\nEX 62\nMvav 68,188,104\nEX 63\nMvav 108,204,104\nEX 100\n"
"Mvav 68,192,144\nEX 101\nMvav 108,208,144\nEX 102\nMaav 8,212,132\nEX 103\n"
"Maav 12,216,132\nCV 140,60,61\nCV 141,62,63\nMaav 48,220,132\nCV 142,100,101\nCV 143,102,103\n"
"Maav 52,224,132\nEX 68\nMaav 88,228,132\nEX 69\nMaav 92,232,132\nEX 70\n"
"Maav 128,236,132\nEX 71\nMaav 132,240,132\nEX 108\nMaav 164,244,132\ns_waitcnt vmcnt(9)\n"
"EX 109\ns_barrier\nCvav 76,180,32,36\nSA m0,s66,50560\nEX 110\nCvav 116,196,32,36\n"
"DL 252,40,70,0\nMvav 76,184,72\nSA m0,s66,51592\nSA s70,s75,131072\nCV 148,68,69\nMvav 116,200,72\n"
"EX 111\nMvav 76,188,112\nCV 149,70,71\nCV 150,108,109\nMvav 116,204,112\nDL 252,40,71,0\n"
"Mvav 76,192,152\nSA m0,s66,52624\nSA s71,s75,196608\nCV 151,110,111\nMvav 116,208,152\nTa 180,246,0\n"
"v_add_u32 v248, s34, v248\nMaav 16,212,140\nTa 182,246,256\nMaav 20,216,140\nDL 252,40,70,0\nMaav 56,220,140\n"
"SA m0,s66,53656\nEX 76\nMaav 60,224,140\nEX 77\nMaav 96,228,140\nEX 78\n"
"Maav 100,232,140\nDL 252,40,71,0\nMaav 136,236,140\nEX 79\nMaav 140,240,140\nEX 116\n"
"Maav 168,244,140\nEX 117\nMaav 24,212,148\nEX 118\nMaav 28,216,148\nEX 119\n"
"Maav 64,220,148\nCV 156,76,77\nCV 157,78,79\nMaav 68,224,148\nCV 158,116,117\nCV 159,118,119\n"
"Maav 104,228,148\nTa 184,246,8\nv_add_u32 v161, 1, v161\nMaav 108,232,148\nTa 186,246,264\nMaav 144,236,148\n"
"Ta 188,246,512\nMaav 148,240,148\nTa 190,246,768\nMaav 172,244,148\nTa 192,246,520\nMaav 32,212,156\n"
"Ta 194,246,776\nMaav 36,216,156\nTa 196,246,4128\nTa 198,246,4384\nTa 200,246,4136\nTa 202,246,4392\n"
"Ta 204,246,4640\nTa 206,246,4896\nTa 208,246,4648\nTa 210,246,4904\nRa 248,245,0\nRa 252,245,2112\n"
"Ra 212,245,4224\nRa 216,245,6336\nMaav 72,220,156\nRa 220,245,8448\nv_add_u32 v245, 0xfffffc00, v245\nMaav 76,224,156\n"
"buffer_store_dword v161, v160, s[56:59], 0 offen offset:64 sc1\nMaav 112,228,156\nMaav 116,232,156\nMaav 152,236,156\nMaav 156,240,156\nMaav 176,244,156\n"
"s_waitcnt lgkmcnt(0)\ns_mov_b32 s90, 10\ns_or_b32 s91, s91, 1\ns_mov_b64 s[88:89], 0\ns_branch RND%=\nPS0%=:\n"
"s_sleep 1\ns_memrealtime s[94:95]\ns_branch PB0%=\nPS1%=:\ns_sleep 1\ns_memrealtime s[94:95]\n"
"s_branch PB1%=\nPS2%=:\ns_sleep 1\ns_memrealtime s[94:95]\ns_branch PB2%=\nPS3%=:\n"
"s_sleep 1\ns_memrealtime s[94:95]\ns_branch PB3%=\nPS4%=:\ns_sleep 1\ns_memrealtime s[94:95]\n"
"s_branch PB4%=\nTL2%=:\nCvav 76,180,32,36\nSA s60,s78,0\nv_and_b32 v252, 0x30, v253\nv_bfe_u32 v254, v253, 9, 1\n"
"Cvav 116,196,32,36\ns_and_b32 s60, s60, 7\nv_lshl_add_u32 v252, v254, 6, v252\nv_bfe_u32 v254, v253, 6, 2\nMvav 76,184,72\ns_lshl_b32 s60, s60, 7\n"
"v_lshl_add_u32 v252, v254, 10, v252\nv_bfe_u32 v254, v253, 8, 1\nMvav 116,200,72\nSA s60,s60,s63\nv_lshl_add_u32 v252, v254, 14, v252\nRa 180,247,0\n"
"Mvav 76,188,112\nSA s70,s60,0\nRa 196,247,1024\nCV 148,68,69\nMvav 116,204,112\nSA m0,s69,130816\n"
"Ra 184,247,256\nCV 149,70,71\nMvav 76,192,152\nDL 252,52,70,0\nMvav 116,208,152\nSA s71,s60,4096\n"
"SA m0,s69,131848\nRa 200,247,1280\nMaav 16,212,140\nSA s70,s60,8192\nRa 188,247,512\nv_add_u32 v245, s85, v253\n"
"Maav 20,216,140\nds_write_b128 v245, v[160:163] offset:1024\nMaav 56,220,140\nDL 252,52,71,0\nMaav 60,224,140\nSA m0,s69,132880\n"
"SA s71,s60,12288\nRa 204,247,1536\nMaav 96,228,140\nSA s33,s33,0x40000000\ns_cselect_b32 s34, s35, -33536\nRa 192,247,768\n"
"v_add_u32 v251, s86, v253\nMaav 100,232,140\nds_write_b128 v245, v[180:183] offset:2048\nMaav 136,236,140\nDL 252,52,70,0\nMaav 140,240,140\n"
"SA m0,s69,133912\nSA s70,s60,32768\nRa 208,247,1792\nMaav 168,244,140\ns_waitcnt lgkmcnt(7)\nEX 108\n"
"Cvav 44,180,0,4\nds_write_b128 v245, v[200:203] offset:3072\nCvav 84,196,0,4\nDL 252,52,71,0\nMvav 44,184,40\ns_waitcnt lgkmcnt(5)\n"
"SA m0,s69,134944\nSA s71,s60,36864\nMvav 84,200,40\nEX 109\nMvav 44,188,80\ns_waitcnt lgkmcnt(4)\n"
"EX 110\nMvav 84,204,80\ns_waitcnt lgkmcnt(3)\nEX 111\nMvav 44,192,120\ns_waitcnt lgkmcnt(1)\n"
"CV 150,108,109\nCV 151,110,111\nMvav 84,208,120\nds_write_b128 v245, v[220:223] offset:4096\nMaav 24,212,148\nDL 252,52,70,0\n"
"Maav 28,216,148\nSA m0,s69,135976\nSA s70,s60,40960\nv_add_u32 v247, s34, v247\nMaav 64,220,148\nEX 76\n"
"Maav 68,224,148\nEX 77\nMaav 104,228,148\nDL 252,52,71,0\nMaav 108,232,148\nSA m0,s69,137008\n"
"SA s71,s60,45056\nCV 156,76,77\nMaav 144,236,148\nEX 78\nMaav 148,240,148\nEX 79\n"
"Maav 172,244,148\nDL 252,52,70,0\nCvav 52,180,8,12\nSA m0,s69,138040\nEX 116\nCvav 92,196,8,12\n"
"EX 117\nMvav 52,184,48\nEX 118\nMvav 92,200,48\ns_getpc_b64 s[94:95]\n2:\n"
"v_lshrrev_b32 v255, 8, v253\ns_lshl_b32 s92, s29, 2\nv_add_lshl_u32 v255, v255, s92, 7\nv_min_u32 v255, END%=-2b-4, v255\nglobal_load_dword v255, v255, s[94:95]\nMvav 52,188,88\n"
"EX 119\nMvav 92,204,88\nCV 157,78,79\nCV 158,116,117\nMvav 52,192,128\nCV 159,118,119\n"
"v_add_u32 v245, 0x1bd00, v245\nMvav 92,208,128\nds_write_b128 v245, v[164:167] offset:0\nMaav 32,212,156\nDL 252,52,71,0\nMaav 36,216,156\n"
"Ta 212,248,0\nMaav 72,220,156\nTa 214,248,1024\nMaav 76,224,156\nds_write_b128 v245, v[184:187] offset:1024\nMaav 112,228,156\n"
"Ta 216,248,32\nMaav 116,232,156\nTa 218,248,1056\nMaav 152,236,156\nTa 220,248,256\nMaav 156,240,156\n"
"ds_write_b128 v245, v[204:207] offset:2048\nMaav 176,244,156\nTa 222,248,1280\nCvav 60,180,16,20\nTa 224,248,288\nCvav 100,196,16,20\n"
"Ta 226,248,1312\nMvav 60,184,56\nds_write_b128 v245, v[224:227] offset:3072\nMvav 100,200,56\nTa 228,248,512\nv_add_u32 v245, 0xffffbd00, v245\n"
"Mvav 60,188,96\nTa 230,248,1536\nMvav 100,204,96\nTa 232,248,544\nTa 234,248,1568\nMvav 60,192,136\n"
"ds_write_b128 v245, v[168:171] offset:0\nEX 44\nEX 45\nEX 46\nEX 47\nEX 84\n"
"EX 85\nEX 86\nEX 87\nTa 236,248,768\nTa 238,248,1792\nMvav 100,208,136\n"
"CV 124,44,45\nCV 125,46,47\nCV 126,84,85\nCV 127,86,87\nTa 240,248,800\nTa 242,248,1824\n"
"s_waitcnt lgkmcnt(15)\nMaav 0,212,124\nEX 52\nMaav 4,216,124\ns_waitcnt lgkmcnt(12)\nEX 53\n"
"Maav 40,220,124\ns_waitcnt lgkmcnt(10)\nEX 54\nMaav 44,224,124\ns_waitcnt lgkmcnt(7)\nEX 55\n"
"Maav 80,228,124\ns_waitcnt lgkmcnt(5)\nEX 92\nMaav 84,232,124\ns_waitcnt lgkmcnt(2)\nEX 93\n"
"Maav 120,236,124\ns_waitcnt lgkmcnt(0)\nEX 94\nMaav 124,240,124\nds_write_b128 v245, v[188:191] offset:1024\nMaav 160,244,124\n"
"EX 95\nCvav 68,180,24,28\nCV 132,52,53\nCV 133,54,55\nCvav 108,196,24,28\nCV 134,92,93\n"
"CV 135,94,95\nMvav 68,184,64\nds_write_b128 v245, v[208:211] offset:2048\nMvav 108,200,64\nEX 60\nMvav 68,188,104\n"
"EX 61\nMvav 108,204,104\nEX 62\nMvav 68,192,144\nds_write_b128 v245, v[228:231] offset:3072\nMvav 108,208,144\n"
"EX 63\nMaav 8,212,132\nEX 100\nMaav 12,216,132\nEX 101\nMaav 48,220,132\n"
"EX 102\nMaav 52,224,132\nEX 103\nMaav 88,228,132\nCV 140,60,61\nCV 141,62,63\n"
"Maav 92,232,132\nCV 142,100,101\nCV 143,102,103\nMaav 128,236,132\nEX 68\nMaav 132,240,132\n"
"EX 69\nMaav 164,244,132\ns_waitcnt vmcnt(9)\nEX 70\ns_barrier\nCvav 76,180,32,36\n"
"SA s60,s78,1\nRa 180,247,0\nCV 148,68,69\nCvav 116,196,32,36\ns_and_b32 s60, s60, 7\nRa 196,247,1024\n"
"v_add_u32 v248, s34, v248\nMvav 76,184,72\ns_lshl_b32 s60, s60, 7\nRa 184,247,256\nv_add_u32 v245, 0xffff7d00, v245\nMvav 116,200,72\n"
"ds_write_b128 v245, v[172:175] offset:0\nMvav 76,188,112\nSA s60,s60,s63\nRa 200,247,1280\nv_add_u32 v246, 0xfffffc00, v246\nMvav 116,204,112\n"
"SA s70,s60,0\nRa 188,247,512\nSA s71,s60,4096\nMvav 76,192,152\nSA s33,s33,0x40000000\ns_cselect_b32 s34, s35, -33536\n"
"Ra 204,247,1536\nSA s80,s78,0\nMvav 116,208,152\nds_write_b128 v245, v[192:195] offset:1024\nMaav 16,212,140\ns_and_b32 s80, s80, 7\n"
"Ra 192,247,768\ns_mul_i32 s80, s80, 163840\nMaav 20,216,140\nSA s80,s80,s64\nRa 208,247,1792\nv_add_u32 v247, s34, v247\n"
"Maav 56,220,140\ns_mov_b64 s[36:37], s[22:23]\nEX 71\nMaav 60,224,140\nds_write_b128 v245, v[212:215] offset:2048\nMaav 96,228,140\n"
"s_and_b32 s37, s37, 0xffff\nEX 108\nMaav 100,232,140\ns_mov_b32 s38, 325760\nEX 109\nMaav 136,236,140\n"
"EX 110\nMaav 140,240,140\nds_write_b128 v245, v[232:235] offset:3072\nMaav 168,244,140\ns_waitcnt lgkmcnt(5)\nEX 111\n"
"Cvav 44,180,0,4\nCV 149,70,71\nCV 150,108,109\nCvav 84,196,0,4\nCV 151,110,111\nv_add_u32 v247, s84, v246\n"
"Mvav 44,184,40\nds_write_b128 v245, v[176:179] offset:17152\nMvav 84,200,40\nEX 76\nMvav 44,188,80\nEX 77\n"
"Mvav 84,204,80\ns_waitcnt lgkmcnt(2)\nEX 78\nMvav 44,192,120\nds_write_b128 v245, v[196:199] offset:18176\nMvav 84,208,120\n"
"EX 79\nMaav 24,212,148\nEX 116\nMaav 28,216,148\nEX 117\nMaav 64,220,148\n"
"ds_write_b128 v245, v[216:219] offset:19200\nMaav 68,224,148\nEX 118\nMaav 104,228,148\nEX 119\nMaav 108,232,148\n"
"CV 156,76,77\nCV 157,78,79\nMaav 144,236,148\nds_write_b128 v245, v[236:239] offset:20224\nMaav 148,240,148\nCV 158,116,117\n"
"CV 159,118,119\nMaav 172,244,148\nEX 44\nCvav 52,180,8,12\nEX 45\nCvav 92,196,8,12\n"
"EX 46\nMvav 52,184,48\nEX 47\nMvav 92,200,48\nEX 84\nMvav 52,188,88\n"
"EX 85\nMvav 92,204,88\nEX 86\nMvav 52,192,128\nEX 87\nMvav 92,208,128\n"
"CV 124,44,45\nCV 125,46,47\nMaav 32,212,156\nTa 212,248,0\nCV 126,84,85\nMaav 36,216,156\n"
"Ta 214,248,1024\nCV 127,86,87\nMaav 72,220,156\nTa 216,248,32\nv_add_u32 v246, s83, v246\nMaav 76,224,156\n"
"Ta 218,248,1056\nv_add_u32 v246, 0x1ff00, v246\nMaav 112,228,156\nTa 220,248,256\nv_add_u32 v247, 0x1ff00, v247\nMaav 116,232,156\n"
"Ta 222,248,1280\nv_add_u32 v245, 0xffff0d00, v245\nMaav 152,236,156\nTa 224,248,288\nMaav 156,240,156\nTa 226,248,1312\n"
"Maav 176,244,156\nTa 228,248,512\nCvav 60,180,16,20\nTa 230,248,1536\nCvav 100,196,16,20\nTa 232,248,544\n"
"Mvav 60,184,56\nTa 234,248,1568\nMvav 100,200,56\nTa 236,248,768\nMvav 60,188,96\nTa 238,248,1792\n"
"Mvav 100,204,96\nTa 240,248,800\nMvav 60,192,136\nTa 242,248,1824\nv_add_u32 v248, s34, v248\nMvav 100,208,136\n"
"s_waitcnt lgkmcnt(9)\nEX 52\nMaav 0,212,124\nEX 53\nMaav 4,216,124\nEX 54\n"
"Maav 40,220,124\ns_waitcnt lgkmcnt(6)\nEX 55\nMaav 44,224,124\nEX 92\nMaav 80,228,124\n"
"s_waitcnt lgkmcnt(4)\nEX 93\nMaav 84,232,124\ns_waitcnt lgkmcnt(2)\nEX 94\nMaav 120,236,124\n"
"s_waitcnt lgkmcnt(0)\nEX 95\nMaav 124,240,124\nCV 132,52,53\nCV 133,54,55\nMaav 160,244,124\n"
"CV 134,92,93\nCV 135,94,95\nCvav 68,180,24,28\nEX 60\nCvav 108,196,24,28\nEX 61\n"
"Mvav 68,184,64\nEX 62\nMvav 108,200,64\nEX 63\nMvav 68,188,104\nEX 100\n"
"Mvav 108,204,104\nEX 101\nMvav 68,192,144\nEX 102\nMvav 108,208,144\nEX 103\n"
"Maav 8,212,132\nCV 140,60,61\nCV 141,62,63\nMaav 12,216,132\nCV 142,100,101\nCV 143,102,103\n"
"Maav 48,220,132\nEX 68\nMaav 52,224,132\nEX 69\nMaav 88,228,132\nEX 70\n"
"Maav 92,232,132\nEX 71\nMaav 128,236,132\nEX 108\nMaav 132,240,132\nEX 109\n"
"Maav 164,244,132\ns_waitcnt vmcnt(0)\nEX 110\ns_barrier\nCvav 76,180,32,36\nSA m0,s69,30208\n"
"EX 111\nCvav 116,196,32,36\nDL 252,52,70,0\nMvav 76,184,72\nSA m0,s69,31240\nSA s70,s60,8192\n"
"CV 148,68,69\nMvav 116,200,72\nCV 149,70,71\nCV 150,108,109\nMvav 76,188,112\nCV 151,110,111\n"
"Ta 180,246,0\nMvav 116,204,112\nDL 252,52,71,0\nMvav 76,192,152\nSA m0,s69,32272\nSA s71,s60,12288\n"
"Ta 182,246,256\nMvav 116,208,152\nTa 184,246,8\nAR 240,160\nMaav 16,212,140\nTa 186,246,264\n"
"v_cmp_ngt_f32 vcc, s77, v240\nMaav 20,216,140\nDL 252,52,70,0\nMaav 56,220,140\nSA m0,s69,33304\nSA s70,s60,32768\n"
"Ta 188,246,4128\nMaav 60,224,140\ns_or_b64 s[88:89], s[88:89], vcc\nEX 76\nMaav 96,228,140\nEX 77\n"
"Maav 100,232,140\nDL 252,52,71,0\nMaav 136,236,140\nSA m0,s69,34336\nSA s71,s60,36864\nCV 156,76,77\n"
"Maav 140,240,140\nEX 78\nMaav 168,244,140\nEX 79\nMaav 24,212,148\nDL 252,52,70,0\n"
"Maav 28,216,148\nSA m0,s69,35368\nSA s70,s60,40960\nCV 157,78,79\nMaav 64,220,148\nEX 116\n"
"Maav 68,224,148\nEX 117\nMaav 104,228,148\nDL 252,52,71,0\nMaav 108,232,148\nSA m0,s69,36400\n"
"SA s71,s60,45056\nSA s60,s78,0\nMaav 144,236,148\ns_and_b32 s60, s60, 7\ns_lshl_b32 s60, s60, 7\nSA s60,s60,s63\n"
"Maav 148,240,148\nEX 118\nEX 119\nMaav 172,244,148\nDL 252,52,70,0\nCV 158,116,117\n"
"CV 159,118,119\nSA m0,s69,37432\nSA s70,s60,131072\nMaav 32,212,156\nTa 190,246,4384\nv_cmp_nle_f32 vcc, 0.5, v240\n"
"Maav 36,216,156\nTa 192,246,4136\nAR 0,0\nMaav 72,220,156\ns_or_b64 s[88:89], s[88:89], vcc\nTa 194,246,4392\n"
"AR 1,1\nMaav 76,224,156\nDL 252,52,71,0\nMaav 112,228,156\nSA s71,s60,135168\nTa 196,246,8256\n"
"AR 2,2\nMaav 116,232,156\nTa 198,246,8512\nAR 3,3\nMaav 152,236,156\nTa 200,246,8264\n"
"AR 254,7\nMaav 156,240,156\nTa 202,246,8520\nAR 40,40\nMaav 176,244,156\ns_waitcnt lgkmcnt(7)\n"
"Ta 204,246,12384\nAR 41,41\nZvav 4,180,160\nTa 206,246,12640\nAR 42,42\nZvav 12,180,164\n"
"Ta 208,246,12392\nAR 43,43\nZvav 20,180,168\nTa 210,246,12648\nAR 255,47\nZvav 28,180,172\n"
"v_rcp_f32 v240, v240\nZvav 36,180,176\nMU 0,v240\nMU 1,v240\nZvav 76,184,176\nMU 2,v240\n"
"MU 3,v240\nZvav 68,184,172\nCV 0,0,1\nCV 1,2,3\nZvav 60,184,168\nAR 2,4\n"
"AR 3,5\nZvav 52,184,164\nRa 0,251,1024\nMU 2,v240\nZvav 44,184,160\ns_waitcnt lgkmcnt(4)\n"
"Ta 180,247,0\nMU 3,v240\nMvav 4,188,180\nTa 182,247,256\nCV 2,2,3\nMvav 12,188,184\n"
"Ta 184,247,8\nAR 3,6\nMvav 20,188,188\nTa 186,247,264\nMU 40,v240\nMvav 28,188,192\n"
"Ra 4,251,2048\nMU 41,v240\nMvav 36,188,196\nTa 188,247,4128\nMU 42,v240\nMvav 76,192,196\n"
"Ta 190,247,4384\nMU 43,v240\nMvav 68,192,192\nCV 40,40,41\nCV 41,42,43\nMvav 60,192,188\n"
"AR 42,44\nAR 43,45\nMvav 52,192,184\nRa 40,251,3072\nMU 42,v240\nMvav 44,192,180\n"
"Ta 192,247,4136\nMU 43,v240\nMvav 4,196,200\nTa 194,247,4392\nCV 42,42,43\nMvav 12,196,204\n"
"AR 43,46\nAR 80,80\nMvav 20,196,208\nAR 81,81\nRa 44,251,4096\nMvav 28,196,212\n"
"MU 80,v240\nMU 81,v240\nMvav 36,196,216\nTa 196,247,8256\nAR 82,82\nMvav 76,200,216\n"
"Ta 198,247,8512\nAR 83,83\nMvav 68,200,212\nMU 82,v240\nMU 83,v240\nMvav 60,200,208\n"
"CV 80,80,81\nCV 81,82,83\nMvav 52,200,204\nv_add_u32 v251, 0x1bd00, v251\nAR 82,84\nMvav 44,200,200\n"
"s_waitcnt lgkmcnt(4)\nTa 200,247,8264\nAR 83,85\nMvav 4,204,220\nTa 202,247,8520\nMU 254,v240\n"
"Mvav 12,204,224\nRa 80,251,0\nMU 3,v240\nMvav 20,204,228\nMU 82,v240\nMU 83,v240\n"
"Mvav 28,204,232\nCV 3,3,254\nCV 82,82,83\nMvav 36,204,236\nTa 204,247,12384\nAR 254,87\n"
"Mvav 76,208,236\nTa 206,247,12640\nAR 83,86\nMvav 68,208,232\nAR 241,164\nAR 120,120\n"
"Mvav 60,208,228\nAR 121,121\nv_cmp_ngt_f32 vcc, s77, v241\nMvav 52,208,224\nRa 84,251,1024\nMU 120,v240\n"
"Mvav 44,208,220\ns_or_b64 s[88:89], s[88:89], vcc\nTa 208,247,12392\nMU 121,v240\nTa 210,247,12648\nMvaa 4,180,0\n"
"AR 122,122\nAR 123,123\nMvaa 44,184,0\nMU 122,v240\nMU 123,v240\nMvaa 4,188,4\n"
"s_waitcnt lgkmcnt(5)\nCV 120,120,121\nv_cmp_nle_f32 vcc, 0.5, v241\nMvaa 44,192,4\nCV 121,122,123\nAR 122,124\n"
"v_rcp_f32 v241, v241\nMvaa 4,196,40\ns_or_b64 s[88:89], s[88:89], vcc\nAR 123,125\nAR 8,8\nAR 9,9\n"
"AR 48,48\nAR 49,49\nRa 120,251,2048\nMU 255,v240\nMU 43,v240\nMU 122,v240\n"
"MU 123,v240\nMU 8,v241\nMU 9,v241\nAR 10,10\nAR 11,11\nMU 48,v241\n"
"MU 49,v241\nAR 50,50\nAR 51,51\nMvaa 44,200,40\ns_waitcnt lgkmcnt(4)\nCV 43,43,255\n"
"CV 122,122,123\nMU 10,v241\nMU 11,v241\nCV 8,8,9\nMU 50,v241\nMU 51,v241\n"
"CV 48,48,49\nAR 255,127\nAR 123,126\nCV 9,10,11\nCV 49,50,51\nMvaa 4,204,44\n"
"s_waitcnt lgkmcnt(1)\nAR 10,12\nAR 11,13\nAR 50,52\nAR 51,53\nRa 124,251,3072\n"
"MU 254,v240\nMU 83,v240\nMU 255,v240\nMU 123,v240\nMU 10,v241\nMU 11,v241\n"
"MU 50,v241\nMU 51,v241\nMvaa 44,208,44\nCV 83,83,254\nCV 123,123,255\nCV 10,10,11\n"
"CV 50,50,51\nAR 254,15\nAR 11,14\nAR 255,55\nAR 51,54\nv_add_u32 v251, 0xffffbd00, v251\n"
"Mvaa 12,180,80\nRa 8,251,0\nAR 88,88\nRa 12,251,1024\nRa 48,251,2048\nRa 52,251,3072\n"
"AR 89,89\nAR 90,90\nAR 91,91\nv_add_u32 v251, 0xffff7d00, v251\nMvaa 52,184,80\nRa 88,251,0\n"
"AR 242,168\nAR 16,16\nAR 17,17\nAR 18,18\nAR 19,19\nMvaa 12,188,84\n"
"v_cmp_ngt_f32 vcc, s77, v242\nRa 16,251,17152\nAR 128,128\nAR 129,129\nMvaa 52,192,84\ns_waitcnt lgkmcnt(7)\n"
"s_or_b64 s[88:89], s[88:89], vcc\nMU 88,v241\nMU 89,v241\nMU 128,v241\nMU 129,v241\nAR 130,130\n"
"AR 131,131\nv_cmp_nle_f32 vcc, 0.5, v242\nMU 90,v241\nMU 91,v241\nCV 88,88,89\nMU 130,v241\n"
"MU 131,v241\nCV 128,128,129\nv_rcp_f32 v242, v242\nMvaa 12,196,120\ns_or_b64 s[88:89], s[88:89], vcc\nCV 89,90,91\n"
"CV 129,130,131\nAR 56,56\nAR 57,57\nAR 90,92\nAR 91,93\nAR 130,132\n"
"AR 131,133\nMU 16,v242\nMU 17,v242\nMU 56,v242\nMU 57,v242\nAR 58,58\n"
"AR 59,59\nMvaa 52,200,120\ns_waitcnt lgkmcnt(6)\nMU 254,v241\nMU 11,v241\nMU 255,v241\n"
"MU 51,v241\nMU 90,v241\nMU 91,v241\nMU 130,v241\nMU 131,v241\nMU 18,v242\n"
"MU 19,v242\nCV 16,16,17\nMU 58,v242\nMU 59,v242\nCV 56,56,57\nCV 11,11,254\n"
"CV 51,51,255\nCV 90,90,91\nCV 130,130,131\nCV 17,18,19\nCV 57,58,59\nMvaa 12,204,124\n"
"AR 254,95\nAR 91,94\nAR 255,135\nAR 131,134\nAR 18,20\nAR 19,21\n"
"AR 58,60\nAR 59,61\nMU 254,v241\nMU 91,v241\nMU 255,v241\nMU 131,v241\n"
"MU 18,v242\nMU 19,v242\nMU 58,v242\nMU 59,v242\nMvaa 52,208,124\ns_waitcnt lgkmcnt(5)\n"
"CV 91,91,254\nCV 131,131,255\nCV 18,18,19\nCV 58,58,59\nRa 92,251,1024\nAR 254,23\n"
"AR 19,22\nAR 255,63\nAR 59,62\nMvaa 20,180,8\ns_waitcnt lgkmcnt(2)\nRa 128,251,2048\n"
"v_add_u32 v246, 0xfffe7700, v246\nRa 132,251,3072\nRa 20,251,18176\nRa 56,251,19200\nRa 60,251,20224\nMvaa 28,180,88\n"
"s_waitcnt lgkmcnt(6)\nv_add_u32 v247, 0xfffe7700, v247\nAR 243,172\nMvaa 36,180,16\nv_cmp_ngt_f32 vcc, s77, v243\nAR 96,96\n"
"Mvaa 60,184,8\ns_or_b64 s[88:89], s[88:89], vcc\nAR 97,97\nAR 136,136\nMvaa 68,184,88\nAR 137,137\n"
"MU 96,v242\nMvaa 76,184,16\nMU 97,v242\nAR 98,98\nMvaa 20,188,12\ns_waitcnt lgkmcnt(5)\n"
"AR 99,99\nMU 136,v242\nMvaa 28,188,92\ns_waitcnt vmcnt(0) lgkmcnt(0)\nMU 137,v242\nAR 138,138\n"
"s_barrier\nMvaa 36,188,20\nds_write_b128 v245, v[0:3] offset:0\nMvaa 60,192,12\nSA m0,s69,130816\nTa 180,246,0\n"
"AR 139,139\nMvaa 68,192,92\nDL 252,52,70,0\nMvaa 76,192,20\nSA m0,s69,131848\nSA s70,s60,139264\n"
"Ta 182,246,256\nMvaa 20,196,48\nds_write_b128 v245, v[40:43] offset:1024\nMvaa 28,196,128\nTa 184,246,8\nv_cmp_nle_f32 vcc, 0.5, v243\n"
"Mvaa 36,196,56\nDL 252,52,71,0\nMvaa 60,200,48\nSA m0,s69,132880\nSA s71,s60,143360\nTa 186,246,264\n"
"Mvaa 68,200,128\nds_write_b128 v245, v[80:83] offset:2048\nMvaa 76,200,56\ns_or_b64 s[88:89], s[88:89], vcc\nTa 188,246,4128\nMU 98,v242\n"
"Mvaa 20,204,52\nDL 252,52,70,0\nMvaa 28,204,132\nSA m0,s69,133912\nSA s70,s60,163840\nTa 190,246,4384\n"
"Mvaa 36,204,60\nds_write_b128 v245, v[120:123] offset:3072\nMvaa 60,208,52\nTa 192,246,4136\nMU 99,v242\nMvaa 68,208,132\n"
"DL 252,52,71,0\nMvaa 76,208,60\ns_waitcnt lgkmcnt(6)\nSA m0,s69,134944\nSA s71,s60,167936\nZvav 84,180,160\n"
"Ta 194,246,4392\nCV 96,96,97\nZvav 92,180,164\nTa 196,246,8256\nMU 138,v242\nZvav 100,180,168\n"
"DL 252,52,70,0\nZvav 108,180,172\nSA m0,s69,135976\nSA s70,s60,172032\nTa 198,246,8512\nZvav 116,180,176\n"
"s_waitcnt lgkmcnt(6)\nTa 200,246,8264\nMU 139,v242\nZvav 156,184,176\nTa 202,246,8520\nCV 136,136,137\n"
"Zvav 148,184,172\nDL 252,52,71,0\nZvav 140,184,168\nSA m0,s69,137008\nSA s71,s60,176128\nSA s60,s78,1\n"
"Zvav 132,184,164\ns_and_b32 s60, s60, 7\ns_lshl_b32 s60, s60, 7\nSA s60,s60,s63\nZvav 124,184,160\ns_waitcnt lgkmcnt(5)\n"
"Ta 204,246,12384\nCV 97,98,99\nMvav 84,188,180\nDL 252,52,70,0\nMvav 92,188,184\nSA m0,s69,138040\n"
"SA s70,s60,131072\nTa 206,246,12640\nMvav 100,188,188\nTa 208,246,12392\nCV 137,138,139\nMvav 108,188,192\n"
"Ta 210,246,12648\nAR 24,24\nMvav 116,188,196\ns_waitcnt lgkmcnt(5)\nTa 180,247,0\nAR 25,25\n"
"Mvav 156,192,196\nDL 252,52,71,0\nMvav 148,192,192\nSA s71,s60,135168\nTa 182,247,256\nAR 64,64\n"
"Mvav 140,192,188\nTa 184,247,8\nAR 65,65\nMvav 132,192,184\nTa 186,247,264\nAR 98,100\n"
"Mvav 124,192,180\nTa 188,247,4128\nAR 99,101\nMvav 84,196,200\nTa 190,247,4384\nAR 138,140\n"
"Mvav 92,196,204\nv_rcp_f32 v243, v243\nMvav 100,196,208\nAR 139,141\nMU 24,v243\nMvav 108,196,212\n"
"MU 25,v243\nAR 26,26\nMvav 116,196,216\ns_waitcnt lgkmcnt(5)\nAR 27,27\nMU 64,v243\n"
"Mvav 156,200,216\nMU 65,v243\nAR 66,66\nMvav 148,200,212\nAR 67,67\nAR 244,176\n"
"Mvav 140,200,208\nMU 254,v242\nMU 19,v242\nMvav 132,200,204\nMU 255,v242\nMU 59,v242\n"
"Mvav 124,200,200\nMU 98,v242\nMU 99,v242\nMvav 84,204,220\nMU 138,v242\nMU 139,v242\n"
"Mvav 92,204,224\nMU 26,v243\nMU 27,v243\nMvav 100,204,228\nCV 24,24,25\nMU 66,v243\n"
"Mvav 108,204,232\nMU 67,v243\nCV 64,64,65\nMvav 116,204,236\nv_cmp_ngt_f32 vcc, s77, v244\nTa 192,247,4136\n"
"Mvav 156,208,236\ns_or_b64 s[88:89], s[88:89], vcc\nTa 194,247,4392\nCV 19,19,254\nMvav 148,208,232\nCV 59,59,255\n"
"CV 98,98,99\nMvav 140,208,228\nCV 138,138,139\nCV 25,26,27\nMvav 132,208,224\nCV 65,66,67\n"
"AR 104,104\nMvav 124,208,220\ns_waitcnt lgkmcnt(2)\nAR 105,105\nAR 144,144\nMvaa 84,180,0\n"
"AR 145,145\nAR 254,103\nMvaa 92,180,80\nAR 99,102\nAR 255,143\nMvaa 100,180,8\n"
"AR 139,142\nAR 26,28\nMvaa 108,180,88\nAR 27,29\nAR 66,68\nMvaa 116,180,16\n"
"AR 67,69\nMU 104,v243\nMvaa 156,184,16\nMU 105,v243\nAR 106,106\nMvaa 148,184,88\n"
"AR 107,107\nMU 144,v243\nMvaa 140,184,8\nMU 145,v243\nAR 146,146\nMvaa 132,184,80\n"
"AR 147,147\nv_cmp_nle_f32 vcc, 0.5, v244\nMU 254,v242\nMU 99,v242\nMU 255,v242\nMU 139,v242\n"
"MU 26,v243\nMU 27,v243\nMU 66,v243\nMU 67,v243\nMU 106,v243\nMU 107,v243\n"
"CV 104,104,105\nMU 146,v243\nMU 147,v243\nCV 144,144,145\nv_rcp_f32 v244, v244\nMvaa 124,184,0\n"
"s_or_b64 s[88:89], s[88:89], vcc\nCV 99,99,254\nCV 139,139,255\nCV 26,26,27\nCV 66,66,67\nCV 105,106,107\n"
"CV 145,146,147\nAR 32,32\nAR 33,33\nAR 72,72\nAR 73,73\nAR 254,31\n"
"AR 27,30\nAR 255,71\nAR 67,70\nAR 106,108\nAR 107,109\nAR 146,148\n"
"AR 147,149\nMU 32,v244\nMU 33,v244\nAR 34,34\nAR 35,35\nMU 72,v244\n"
"MU 73,v244\nAR 74,74\nAR 75,75\nMvaa 84,188,4\ns_or_b32 s72, s88, s89\nMU 254,v243\n"
"MU 27,v243\nMU 255,v243\nMU 67,v243\nMU 106,v243\nMU 107,v243\nMU 146,v243\n"
"MU 147,v243\nMU 34,v244\nMU 35,v244\nCV 32,32,33\nMU 74,v244\nMU 75,v244\n"
"CV 72,72,73\nCV 27,27,254\nCV 67,67,255\nCV 106,106,107\nCV 146,146,147\nCV 33,34,35\n"
"CV 73,74,75\nAR 112,112\nAR 113,113\nAR 152,152\nAR 153,153\nMvaa 92,188,84\n"
"AR 254,111\nAR 107,110\nAR 255,151\nAR 147,150\nAR 34,36\nAR 35,37\n"
"AR 74,76\nAR 75,77\nMU 112,v244\nMU 113,v244\nAR 114,114\nAR 115,115\n"
"MU 152,v244\nMU 153,v244\nAR 154,154\nAR 155,155\nMU 254,v243\nMU 107,v243\n"
"MU 255,v243\nMU 147,v243\nMU 34,v244\nMU 35,v244\nMU 74,v244\nMU 75,v244\n"
"MU 114,v244\nMU 115,v244\nCV 112,112,113\nMU 154,v244\nMU 155,v244\nCV 152,152,153\n"
"Mvaa 100,188,12\nCV 107,107,254\nCV 147,147,255\nCV 34,34,35\nCV 74,74,75\nCV 113,114,115\n"
"CV 153,154,155\nTa 196,247,8256\nTa 198,247,8512\nAR 254,39\nAR 35,38\nAR 255,79\n"
"AR 75,78\nAR 114,116\nAR 115,117\nAR 154,156\nAR 155,157\nMvaa 108,188,92\n"
"MU 254,v244\nMU 35,v244\nMU 255,v244\nMU 75,v244\nMU 114,v244\nMU 115,v244\n"
"MU 154,v244\nMU 155,v244\nCV 35,35,254\nCV 75,75,255\nCV 114,114,115\nCV 154,154,155\n"
"Mvaa 116,188,20\ns_waitcnt lgkmcnt(2)\nAR 254,119\nAR 115,118\nAR 255,159\nAR 155,158\n"
"MU 254,v244\nMU 115,v244\nMU 255,v244\nMU 155,v244\nMvaa 156,192,20\nCV 115,115,254\n"
"CV 155,155,255\nv_add_u32 v245, 0xf300, v245\nv_mov_b32 v254, s72\nv_mov_b32 v255, s99\nMvaa 148,192,92\nds_write_b128 v245, v[8:11] offset:0\n"
"ds_write_b128 v245, v[48:51] offset:1024\nds_write_b128 v245, v[88:91] offset:2048\nds_write_b128 v245, v[128:131] offset:3072\nds_write_b128 v245, v[16:19] offset:51200\nds_write_b128 v245, v[56:59] offset:52224\nds_write_b128 v245, v[96:99] offset:53248\n"
"ds_write_b128 v245, v[136:139] offset:54272\nds_write_b128 v245, v[24:27] offset:34304\nds_write_b128 v245, v[64:67] offset:35328\nds_write_b128 v245, v[104:107] offset:36352\nds_write_b128 v245, v[144:147] offset:37376\nds_write_b128 v245, v[32:35] offset:17152\n"
"ds_write_b128 v245, v[72:75] offset:18176\nds_write_b128 v245, v[112:115] offset:19200\nds_write_b128 v245, v[152:155] offset:20224\nTa 200,247,8264\nTa 202,247,8520\nTa 204,247,12384\n"
"Ta 206,247,12640\nTa 208,247,12392\nTa 210,247,12648\nds_write_b32 v255, v254\nMvaa 140,192,12\nSA s72,s80,4096\n"
"v_add_u32 v246, 0x18900, v246\nv_add_u32 v251, 0xffff0d00, v251\nMvaa 132,192,84\nv_add_u32 v247, 0x18900, v247\nv_mov_b32 v254, %[lds]\nMvaa 124,192,4\n"
"s_waitcnt lgkmcnt(15)\nv_lshlrev_b32 v248, 5, v253\nv_cmp_gt_u32 vcc, 0x70, v253\nMvaa 84,196,40\nv_mov_b32 v255, 104857600\nv_cndmask_b32 v248, v255, v248, vcc\n"
"Mvaa 92,196,120\nv_lshl_add_u32 v255, v253, 8, s96\nv_add_u32 v255, 0x1000, v255\nMvaa 100,196,48\nv_add_u32 v248, s87, v248\nv_and_b32 v255, 0x7000, v255\n"
"Mvaa 108,196,128\ns_waitcnt vmcnt(0) lgkmcnt(0)\nv_add_u32 v250, v255, v248\nv_add_u32 v248, s96, v248\ns_barrier\nMvaa 116,196,56\n"
"SA m0,s69,30208\nTa 180,246,0\nv_mov_b32 v244, 0\nMvaa 156,200,56\nDL 252,52,70,0\nMvaa 148,200,128\n"
"SA m0,s69,31240\nSA s70,s60,139264\nTa 182,246,256\nMvaa 140,200,48\nTa 184,246,8\nv_mov_b32 v245, 0\n"
"Mvaa 132,200,120\nTa 186,246,264\nMvaa 124,200,40\nDL 252,52,71,0\nMvaa 84,204,44\nSA m0,s69,32272\n"
"SA s71,s60,143360\nTa 188,246,4128\nMvaa 92,204,124\nTa 190,246,4384\nMvaa 100,204,52\nTa 192,246,4136\n"
"Mvaa 108,204,132\nDL 252,52,70,0\nMvaa 116,204,60\nSA m0,s69,33304\nSA s70,s60,163840\nTa 194,246,4392\n"
"Mvaa 156,208,60\nTa 196,246,8256\nMvaa 148,208,132\nTa 198,246,8512\nMvaa 140,208,52\nDL 252,52,71,0\n"
"Mvaa 132,208,124\nSA m0,s69,34336\nSA s71,s60,167936\nTa 200,246,8264\nMvaa 124,208,44\ns_waitcnt lgkmcnt(7)\n"
"Ta 202,246,8520\nMvav 4,180,0\nTa 204,246,12384\nMvav 12,180,8\nDL 252,52,70,0\nMvav 20,180,16\n"
"SA m0,s69,35368\nSA s70,s60,172032\nTa 206,246,12640\nMvav 28,180,24\nTa 208,246,12392\nMvav 36,180,32\n"
"Ta 210,246,12648\nv_add_u32 v246, 0xfffe7700, v246\nMvav 76,184,32\nDL 252,52,71,0\nMvav 68,184,24\nSA m0,s69,36400\n"
"SA s71,s60,176128\nSA s60,s78,2\nMvav 60,184,16\ns_and_b32 s60, s60, 7\ns_lshl_b32 s60, s60, 7\nSA s60,s60,s63\n"
"Mvav 52,184,8\nRa 96,251,0\nMvav 44,184,0\ns_waitcnt lgkmcnt(5)\nRa 100,251,1024\nMvav 4,188,40\n"
"DL 252,52,70,0\nMvav 12,188,48\nSA m0,s69,37432\nSA s70,s60,0\nRa 136,251,2048\nMvav 20,188,56\n"
"Ra 140,251,3072\nv_add_u32 v251, 0xf300, v251\nMvav 28,188,64\nTa 180,247,0\nMvav 36,188,72\nDL 252,52,71,0\n"
"Mvav 76,192,72\nSA s71,s60,4096\nTa 182,247,256\nMvav 68,192,64\nRa 24,251,0\nMvav 60,192,56\n"
"Ra 104,251,51200\nMvav 52,192,48\nRa 32,251,34304\nMvav 44,192,40\nRa 112,251,17152\nMvav 4,196,80\n"
"Ta 184,247,8\nMvav 12,196,88\nTa 186,247,264\nMvav 20,196,96\nTa 188,247,4128\nMvav 28,196,104\n"
"Ta 190,247,4384\nMvav 36,196,112\nRa 28,251,1024\nMvav 76,200,112\nRa 108,251,52224\nMvav 68,200,104\n"
"Ra 36,251,35328\nMvav 60,200,96\nRa 116,251,18176\nMvav 52,200,88\nTa 192,247,4136\nMvav 44,200,80\n"
"s_waitcnt lgkmcnt(9)\nTa 194,247,4392\nMvav 4,204,120\nTa 196,247,8256\nMvav 12,204,128\nTa 198,247,8512\n"
"Mvav 20,204,136\nRa 64,251,2048\nMvav 28,204,144\nRa 144,251,53248\nMvav 36,204,152\nRa 72,251,36352\n"
"Mvav 76,208,152\nRa 68,251,3072\nMvav 68,208,144\nRa 148,251,54272\nMvav 60,208,136\nRa 76,251,37376\n"
"Mvav 52,208,128\nRa 152,251,19200\nMvav 44,208,120\nRa 156,251,20224\nMvaa 4,180,96\nbuffer_load_dword v251, v248, s[56:59], 0 offen offset:64 sc1\n"
"Mvaa 12,180,24\nTa 200,247,8264\nMvaa 20,180,104\nTa 202,247,8520\nMvaa 28,180,32\nTa 204,247,12384\n"
"Mvaa 36,180,112\ns_waitcnt lgkmcnt(8)\nTa 206,247,12640\nMvaa 76,184,112\nTa 208,247,12392\nMvaa 68,184,32\n"
"Ta 210,247,12648\nv_add_u32 v247, 0xfffe7700, v247\nMvaa 60,184,104\nds_read_b128 v[240:243], v254\nMvaa 52,184,24\nbuffer_load_dword v254, v250, s[56:59], 0 offen offset:64 sc1\n"
"Mvaa 44,184,96\nMvaa 4,188,100\nMvaa 12,188,28\nMvaa 20,188,108\nMvaa 28,188,36\nMvaa 36,188,116\n"
"Mvaa 76,192,116\ns_waitcnt lgkmcnt(0)\nv_or3_b32 v240, v240, v241, v242\nv_or_b32 v240, v240, v243\nMvaa 68,192,36\nv_readfirstlane_b32 s88, v240\n"
"v_lshrrev_b32 v240, 8, v253\nMvaa 60,192,108\nv_bfe_u32 v241, v253, 6, 2\nv_cmp_eq_u32 vcc, v240, v241\nMvaa 52,192,28\nv_mov_b32 v240, 0x3f80\n"
"v_cndmask_b32 v240, 0, v240, vcc\nMvaa 44,192,100\nv_and_b32 v241, 16, v253\nv_lshlrev_b32 v240, v241, v240\nMvaa 4,196,136\nv_and_b32 v241, 32, v253\n"
"v_cmp_eq_u32 vcc, 0, v241\nMvaa 12,196,64\nv_mov_b32 v241, 0\nv_cndmask_b32 v242, 0, v240, vcc\nMvaa 20,196,144\nv_sub_u32 v243, v240, v242\n"
"v_mov_b32 v240, 0\nMvaa 28,196,72\ns_waitcnt vmcnt(2)\ns_barrier\nMvaa 36,196,152\nSA m0,s69,130816\n"
"Ta 180,246,0\nMvaa 76,200,152\nDL 252,52,70,0\nMvaa 68,200,72\nSA m0,s69,131848\nSA s70,s60,8192\n"
"Ta 182,246,256\nMvaa 60,200,144\nTa 184,246,8\nMvaa 52,200,64\nTa 186,246,264\nMvaa 44,200,136\n"
"DL 252,52,71,0\nMvaa 4,204,140\nSA m0,s69,132880\nSA s71,s60,12288\nTa 188,246,4128\nMvaa 12,204,68\n"
"Ta 190,246,4384\nMvaa 20,204,148\nTa 192,246,4136\nMvaa 28,204,76\nDL 252,52,70,0\nCV 4,4,5\n"
"CV 5,6,7\nMvaa 36,204,156\nSA m0,s69,133912\nSA s70,s60,32768\nCV 12,12,13\nCV 20,20,21\n"
"CV 28,28,29\nCV 13,14,15\nCV 21,22,23\nCV 29,30,31\nCV 36,36,37\nCV 37,38,39\n"
"Mvaa 76,208,156\nTa 194,246,4392\nMvaa 68,208,76\nTa 196,246,8256\nMvaa 60,208,148\nDL 252,52,71,0\n"
"Mvaa 52,208,68\nSA m0,s69,134944\nSA s71,s60,36864\nCV 38,76,77\nCV 39,78,79\nCV 30,68,69\n"
"CV 31,70,71\nCV 22,60,61\nCV 23,62,63\nCV 14,52,53\nCV 15,54,55\nMvaa 44,208,140\n"
"s_waitcnt lgkmcnt(5)\nTa 198,246,8512\nMvav 84,180,0\nTa 200,246,8264\nMvav 92,180,8\nDL 252,52,70,0\n"
"Mvav 100,180,16\nSA m0,s69,135976\nSA s70,s60,40960\nCV 6,44,45\nCV 7,46,47\nMvav 108,180,24\n"
"Ta 202,246,8520\nMvav 116,180,32\nTa 204,246,12384\nMvav 156,184,32\nDL 252,52,71,0\nMvav 148,184,24\n"
"SA m0,s69,137008\nSA s71,s60,45056\nTa 206,246,12640\nMvav 140,184,16\nTa 208,246,12392\nMvav 132,184,8\n"
"Ta 210,246,12648\nv_add_u32 v246, 0x18900, v246\nMvav 124,184,0\ns_waitcnt lgkmcnt(6)\nTa 180,247,0\nMvav 84,188,40\n"
"DL 252,52,70,0\nMvav 92,188,48\nSA m0,s69,138040\nSA s70,s60,131072\nTa 182,247,256\nMvav 100,188,56\n"
"Ta 184,247,8\nMvav 108,188,64\nTa 186,247,264\nMvav 116,188,72\nDL 252,52,71,0\nMvav 156,192,72\n"
"SA m0,s69,96768\nSA s71,s60,135168\nTa 188,247,4128\nMvav 148,192,64\nTa 190,247,4384\nMvav 140,192,56\n"
"Mvav 132,192,48\nbuffer_store_dwordx4 v[4:7], v253, s[56:59], s80 offen sc1\nMvav 124,192,40\nTa 192,247,4136\nMvav 84,196,80\nTa 194,247,4392\n"
"Mvav 92,196,88\nMvav 100,196,96\nbuffer_store_dwordx4 v[12:15], v253, s[56:59], s80 offen offset:1024 sc1\nMvav 108,196,104\nMvav 116,196,112\ns_waitcnt lgkmcnt(4)\n"
"Ta 196,247,8256\nMvav 156,200,112\nTa 198,247,8512\nMvav 148,200,104\nbuffer_store_dwordx4 v[20:23], v253, s[56:59], s80 offen offset:2048 sc1\nMvav 140,200,96\n"
"Mvav 132,200,88\nMvav 124,200,80\nTa 200,247,8264\nMvav 84,204,120\nbuffer_store_dwordx4 v[28:31], v253, s[56:59], s80 offen offset:3072 sc1\nMvav 92,204,128\n"
"SA s80,s78,1\nTa 202,247,8520\ns_and_b32 s80, s80, 7\nMvav 100,204,136\ns_mul_i32 s80, s80, 163840\nSA s80,s80,s64\n"
"Mvav 108,204,144\nMvav 116,204,152\nbuffer_store_dwordx4 v[36:39], v253, s[56:59], s72 offen sc1\nMvav 156,208,152\nSA s72,s80,4096\nTa 204,247,12384\n"
"Mvav 148,208,144\nTa 206,247,12640\nMvav 140,208,136\nMvav 132,208,128\nDL 252,52,70,0\nMvav 124,208,120\n"
"SA m0,s69,97800\nSA s70,s60,139264\nTa 208,247,12392\nMvaa 84,180,96\nTa 210,247,12648\nv_add_u32 v247, 0x18900, v247\n"
"Mvaa 92,180,24\nMvaa 100,180,104\nDL 252,52,71,0\nMvaa 108,180,32\nSA m0,s69,98832\nSA s71,s60,143360\n"
"Mvaa 116,180,112\nMvaa 156,184,112\nMvaa 148,184,32\nDL 252,52,70,0\nMvaa 140,184,104\nSA m0,s69,99864\n"
"SA s70,s60,163840\nMvaa 132,184,24\nMvaa 124,184,96\ns_waitcnt lgkmcnt(1)\nMvaa 84,188,100\nDL 252,52,71,0\n"
"Mvaa 92,188,28\nSA m0,s69,100896\nSA s71,s60,167936\nMvaa 100,188,108\nMvaa 108,188,36\nMvaa 116,188,116\n"
"DL 252,52,70,0\nMvaa 156,192,116\nSA m0,s69,101928\nSA s70,s60,172032\nMvaa 148,192,36\nMvaa 140,192,108\n"
"Mvaa 132,192,28\nDL 252,52,71,0\nMvaa 124,192,100\nSA m0,s69,102960\nSA s71,s60,176128\nSA s60,s78,3\n"
"Mvaa 84,196,136\ns_and_b32 s60, s60, 7\ns_lshl_b32 s60, s60, 7\nSA s60,s60,s63\nMvaa 92,196,64\nMvaa 100,196,144\n"
"DL 252,52,70,0\nMvaa 108,196,72\nSA m0,s69,103992\nSA s70,s60,0\ns_cmp_lg_u32 s88, 0\ns_cbranch_scc1 FE%=\n"
"s_waitcnt vmcnt(12) lgkmcnt(0)\ns_barrier\nMvaa 116,196,152\nDL 252,52,71,0\nMvaa 156,200,152\nSA m0,s69,1024\n"
"SA s71,s60,4096\nTa 180,246,0\nMvaa 148,200,72\nTa 182,246,256\nMvaa 140,200,144\nTa 184,246,8\n"
"Mvaa 132,200,64\nDL 252,52,70,0\nMvaa 124,200,136\nSA m0,s69,2056\nSA s70,s60,8192\nTa 186,246,264\n"
"Mvaa 84,204,140\nTa 188,246,4128\nMvaa 92,204,68\nTa 190,246,4384\nMvaa 100,204,148\nDL 252,52,71,0\n"
"Mvaa 108,204,76\nSA m0,s69,3088\nSA s71,s60,12288\nCV 84,84,85\nCV 85,86,87\nCV 92,92,93\n"
"CV 93,94,95\nCV 100,100,101\nCV 101,102,103\nCV 108,108,109\nCV 109,110,111\nMvaa 116,204,156\n"
"Ta 192,246,4136\nMvaa 156,208,156\nTa 194,246,4392\nMvaa 148,208,76\nDL 252,52,70,0\nMvaa 140,208,148\n"
"SA m0,s69,4120\nSA s70,s60,32768\nCV 116,116,117\nCV 117,118,119\nCV 118,156,157\nCV 119,158,159\n"
"CV 110,148,149\nCV 111,150,151\nCV 102,140,141\nCV 103,142,143\nMvaa 132,208,68\nTa 196,246,8256\n"
"Mvaa 124,208,140\ns_waitcnt lgkmcnt(5)\nTa 198,246,8512\nZvav 4,180,160\nbuffer_store_dwordx4 v[100:103], v253, s[56:59], s80 offen offset:2048 sc1\nZvav 12,180,164\n"
"Ta 200,246,8264\nCV 94,132,133\nCV 95,134,135\nCV 86,124,125\nCV 87,126,127\nZvav 20,180,168\n"
"Ta 202,246,8520\nZvav 28,180,172\nTa 204,246,12384\nZvav 36,180,176\nbuffer_store_dwordx4 v[84:87], v253, s[56:59], s80 offen sc1\nZvav 76,184,176\n"
"Ta 206,246,12640\nZvav 68,184,172\nTa 208,246,12392\nZvav 60,184,168\nTa 210,246,12648\nv_add_u32 v246, 0xffff7b00, v246\n"
"Zvav 52,184,164\nbuffer_store_dwordx4 v[92:95], v253, s[56:59], s80 offen offset:1024 sc1\nZvav 44,184,160\ns_waitcnt lgkmcnt(6)\nTa 180,247,0\nMvav 4,188,180\n"
"Ta 182,247,256\nMvav 12,188,184\nTa 184,247,8\nMvav 20,188,188\nbuffer_store_dwordx4 v[108:111], v253, s[56:59], s80 offen offset:3072 sc1\nMvav 28,188,192\n"
"SA s80,s78,2\nTa 186,247,264\ns_and_b32 s80, s80, 7\nMvav 36,188,196\ns_mul_i32 s80, s80, 163840\nTa 188,247,4128\n"
"SA s80,s80,s64\nMvav 76,192,196\nTa 190,247,4384\nMvav 68,192,192\nbuffer_store_dwordx4 v[116:119], v253, s[56:59], s72 offen sc1\nMvav 60,192,188\n"
"SA s72,s80,4096\nMvav 52,192,184\nMvav 44,192,180\nTa 192,247,4136\nMvav 4,196,200\nDL 252,52,71,0\n"
"Mvav 12,196,204\nSA m0,s69,5152\nSA s71,s60,36864\nTa 194,247,4392\nMvav 20,196,208\nMvav 28,196,212\n"
"Mvav 36,196,216\ns_waitcnt lgkmcnt(3)\nTa 196,247,8256\nMvav 76,200,216\nDL 252,52,70,0\nMvav 68,200,212\n"
"SA m0,s69,6184\nSA s70,s60,40960\nTa 198,247,8512\nMvav 60,200,208\nMvav 52,200,204\nMvav 44,200,200\n"
"DL 252,52,71,0\nMvav 4,204,220\nSA m0,s69,7216\nSA s71,s60,45056\nTa 200,247,8264\nMvav 12,204,224\n"
"Ta 202,247,8520\nMvav 20,204,228\nMvav 28,204,232\nDL 252,52,70,0\nMvav 36,204,236\nSA m0,s69,8248\n"
"Ta 204,247,12384\nSA s70,s60,131072\nMvav 76,208,236\nTa 206,247,12640\nMvav 68,208,232\nMvav 60,208,228\n"
"DL 252,52,71,0\nMvav 52,208,224\nSA s71,s60,135168\nMvav 44,208,220\ns_waitcnt vmcnt(21)\nTa 208,247,12392\n"
"s_nop 0\nMvaa 4,180,0\ns_mov_b64 exec, 1\nbuffer_store_dword v254, v250, s[56:59], 0 offen sc1\ns_mov_b64 exec, -1\nMvaa 12,180,80\n"
"Ta 210,247,12648\nv_add_u32 v247, 0xffff7b00, v247\nMvaa 20,180,8\nMvaa 28,180,88\nMvaa 36,180,16\nMvaa 76,184,16\n"
"Mvaa 68,184,88\nMvaa 60,184,8\nMvaa 52,184,80\nMvaa 44,184,0\ns_waitcnt lgkmcnt(1)\nMvaa 4,188,4\n"
"Mvaa 12,188,84\nMvaa 20,188,12\nMvaa 28,188,92\nMvaa 36,188,20\nMvaa 76,192,20\nMvaa 68,192,92\n"
"Mvaa 60,192,12\nMvaa 52,192,84\nMvaa 44,192,4\nMvaa 4,196,40\nMvaa 12,196,120\nMvaa 20,196,48\n"
"Mvaa 28,196,128\ns_waitcnt vmcnt(14) lgkmcnt(0)\ns_barrier\nMvaa 36,196,56\nSA m0,s69,130816\nTa 180,246,0\n"
"Mvaa 76,200,56\nDL 252,52,70,0\nMvaa 68,200,128\nSA m0,s69,131848\nSA s70,s60,139264\nTa 182,246,256\n"
"Mvaa 60,200,48\nTa 184,246,8\nMvaa 52,200,120\nTa 186,246,264\nMvaa 44,200,40\nDL 252,52,71,0\n"
"Mvaa 4,204,44\nSA m0,s69,132880\nSA s71,s60,143360\nTa 188,246,4128\nMvaa 12,204,124\nTa 190,246,4384\n"
"Mvaa 20,204,52\nTa 192,246,4136\nMvaa 28,204,132\nDL 252,52,70,0\nMvaa 36,204,60\nSA m0,s69,133912\n"
"SA s70,s60,163840\nTa 194,246,4392\nMvaa 76,208,60\nTa 196,246,8256\nMvaa 68,208,132\nTa 198,246,8512\n"
"Mvaa 60,208,52\nDL 252,52,71,0\nMvaa 52,208,124\nSA m0,s69,134944\nSA s71,s60,167936\nTa 200,246,8264\n"
"Mvaa 44,208,44\ns_waitcnt lgkmcnt(7)\nTa 202,246,8520\nMvav 4,180,0\nTa 204,246,12384\nMvav 12,180,8\n"
"DL 252,52,70,0\nMvav 20,180,16\nSA m0,s69,135976\nSA s70,s60,172032\nTa 206,246,12640\nMvav 28,180,24\n"
"Ta 208,246,12392\nMvav 36,180,32\nTa 210,246,12648\nv_add_u32 v246, 0xfffe8a00, v246\nMvav 76,184,32\nDL 252,52,71,0\n"
"Mvav 68,184,24\nSA m0,s69,137008\nSA s71,s60,176128\nTa 180,247,0\nMvav 60,184,16\nSA s60,s78,4\n"
"Ta 182,247,256\ns_and_b32 s60, s60, 7\nMvav 52,184,8\ns_lshl_b32 s60, s60, 7\nSA s60,s60,s63\nMvav 44,184,0\n"
"s_waitcnt lgkmcnt(6)\nTa 184,247,8\nMvav 4,188,40\nDL 252,52,70,0\nMvav 12,188,48\nSA m0,s69,138040\n"
"Ta 186,247,264\nSA s70,s60,0\nMvav 20,188,56\nMvav 28,188,64\nMvav 36,188,72\nDL 252,52,71,0\n"
"Mvav 76,192,72\nSA s71,s60,4096\nTa 188,247,4128\nMvav 68,192,64\nTa 190,247,4384\nMvav 60,192,56\n"
"Mvav 52,192,48\nMvav 44,192,40\nTa 192,247,4136\nMvav 4,196,80\nTa 194,247,4392\nMvav 12,196,88\n"
"Mvav 20,196,96\nMvav 28,196,104\nMvav 36,196,112\nTa 196,247,8256\nMvav 76,200,112\nTa 198,247,8512\n"
"Mvav 68,200,104\nMvav 60,200,96\nMvav 52,200,88\nMvav 44,200,80\ns_waitcnt lgkmcnt(3)\nTa 200,247,8264\n"
"Mvav 4,204,120\nTa 202,247,8520\nMvav 12,204,128\nMvav 20,204,136\nMvav 28,204,144\nMvav 36,204,152\n"
"Ta 204,247,12384\nMvav 76,208,152\nTa 206,247,12640\nMvav 68,208,144\nMvav 60,208,136\nMvav 52,208,128\n"
"Mvav 44,208,120\ns_waitcnt vmcnt(14)\nTa 208,247,12392\ns_nop 0\nMvaa 4,180,96\nbuffer_load_dword v255, v248, s[56:59], 0 offen sc1\n"
"Mvaa 12,180,24\nTa 210,247,12648\nv_add_u32 v247, 0xfffe8a00, v247\nMvaa 20,180,104\nMvaa 28,180,32\nMvaa 36,180,112\n"
"s_mov_b64 exec, 2\nbuffer_store_dword v254, v250, s[56:59], 0 offen sc1\ns_mov_b64 exec, -1\nMvaa 76,184,112\nMvaa 68,184,32\nMvaa 60,184,104\n"
"Mvaa 52,184,24\nMvaa 44,184,96\nMvaa 4,188,100\nMvaa 12,188,28\nMvaa 20,188,108\nMvaa 28,188,36\n"
"Mvaa 36,188,116\ns_waitcnt lgkmcnt(0)\nMvaa 76,192,116\nMvaa 68,192,36\nMvaa 60,192,108\nMvaa 52,192,28\n"
"Mvaa 44,192,100\nMvaa 4,196,136\nMvaa 12,196,64\nMvaa 20,196,144\nMvaa 28,196,72\ns_waitcnt vmcnt(11)\n"
"s_barrier\nMvaa 36,196,152\nSA m0,s69,79616\nTa 180,246,0\nMvaa 76,200,152\nTa 182,246,256\n"
"Mvaa 68,200,72\nDL 252,52,70,0\nMvaa 60,200,144\nSA m0,s69,80648\nTa 184,246,8\nSA s70,s60,8192\n"
"Mvaa 52,200,64\nTa 186,246,264\nMvaa 44,200,136\nTa 188,246,4128\nMvaa 4,204,140\nDL 252,52,71,0\n"
"Mvaa 12,204,68\nSA m0,s69,81680\nTa 190,246,4384\nSA s71,s60,12288\nMvaa 20,204,148\nTa 192,246,4136\n"
"Mvaa 28,204,76\nCV 4,4,5\nCV 5,6,7\nCV 12,12,13\nCV 13,14,15\nMvaa 36,204,156\n"
"DL 252,52,70,0\nCV 20,20,21\nCV 21,22,23\nCV 28,28,29\nCV 29,30,31\nMvaa 76,208,156\n"
"SA m0,s69,82712\nTa 194,246,4392\nCV 36,36,37\nCV 37,38,39\nMvaa 68,208,76\nSA s70,s60,32768\n"
"Ta 196,246,8256\nMvaa 60,208,148\nCV 38,76,77\nCV 39,78,79\nMvaa 52,208,68\nbuffer_store_dwordx4 v[36:39], v253, s[56:59], s72 offen sc1\n"
"Mvaa 44,208,140\ns_waitcnt lgkmcnt(4)\nCV 30,68,69\nCV 31,70,71\nCV 22,60,61\nCV 23,62,63\n"
"Zvav 84,180,160\nCV 14,52,53\nCV 15,54,55\nCV 6,44,45\nCV 7,46,47\nZvav 92,180,164\n"
"Ta 198,246,8512\nZvav 100,180,168\nbuffer_store_dwordx4 v[4:7], v253, s[56:59], s80 offen sc1\nZvav 108,180,172\nTa 200,246,8264\nZvav 116,180,176\n"
"Ta 202,246,8520\nZvav 156,184,176\nTa 204,246,12384\nZvav 148,184,172\nbuffer_store_dwordx4 v[12:15], v253, s[56:59], s80 offen offset:1024 sc1\nZvav 140,184,168\n"
"s_waitcnt vmcnt(7)\nv_cmp_ne_u32 vcc, v255, v251\nTa 206,246,12640\nZvav 132,184,164\nTa 208,246,12392\nZvav 124,184,160\n"
"Ta 210,246,12648\nv_add_u32 v246, 0x1fb00, v246\ns_and_b32 s73, vcc_lo, 1\ns_cbranch_scc1 STX0%=\nBKX0%=:\ns_waitcnt lgkmcnt(10)\n"
"Mvav 84,188,180\nbuffer_store_dwordx4 v[20:23], v253, s[56:59], s80 offen offset:2048 sc1\nMvav 92,188,184\nSA s81,s24,7\ns_and_b32 s81, s81, 7\ns_mul_i32 s81, s81, 20480\n"
"Mvav 100,188,188\nSA s81,s81,s97\nTa 180,247,0\nMvav 108,188,192\nTa 182,247,256\nMvav 116,188,196\n"
"s_waitcnt lgkmcnt(5)\nTa 184,247,8\nMvav 156,192,196\nbuffer_load_dwordx4 a[160:163], v253, s[56:59], s81 offen sc1\nMvav 148,192,192\nTa 186,247,264\n"
"Mvav 140,192,188\nTa 188,247,4128\nMvav 132,192,184\nTa 190,247,4384\nMvav 124,192,180\nbuffer_load_dwordx4 a[164:167], v253, s[56:59], s81 offen offset:1024 sc1\n"
"Mvav 84,196,200\nTa 192,247,4136\nMvav 92,196,204\nTa 194,247,4392\nMvav 100,196,208\nMvav 108,196,212\n"
"buffer_load_dwordx4 a[168:171], v253, s[56:59], s81 offen offset:2048 sc1\nMvav 116,196,216\nTa 196,247,8256\nMvav 156,200,216\nTa 198,247,8512\nMvav 148,200,212\n"
"Mvav 140,200,208\nbuffer_load_dwordx4 a[172:175], v253, s[56:59], s81 offen offset:3072 sc1\nMvav 132,200,204\nSA s81,s81,4096\nMvav 124,200,200\ns_waitcnt lgkmcnt(4)\n"
"Ta 200,247,8264\nMvav 84,204,220\nTa 202,247,8520\nMvav 92,204,224\nbuffer_load_dwordx4 a[176:179], v253, s[56:59], s81 offen sc1\nMvav 100,204,228\n"
"Mvav 108,204,232\nMvav 116,204,236\nTa 204,247,12384\nMvav 156,208,236\nbuffer_store_dwordx4 v[28:31], v253, s[56:59], s80 offen offset:3072 sc1\nMvav 148,208,232\n"
"SA s80,s78,3\nTa 206,247,12640\ns_and_b32 s80, s80, 7\nMvav 140,208,228\ns_mul_i32 s80, s80, 163840\nSA s80,s80,s64\n"
"SA s72,s80,4096\nMvav 132,208,224\nMvav 124,208,220\nDL 252,52,71,0\nMvaa 84,180,0\nSA m0,s69,83744\n"
"SA s71,s60,36864\nTa 208,247,12392\nMvaa 92,180,80\nTa 210,247,12648\nv_add_u32 v247, 0x1fb00, v247\nMvaa 100,180,8\n"
"Mvaa 108,180,88\nbuffer_load_dword v255, v248, s[56:59], 0 offen sc1\nMvaa 116,180,16\nMvaa 156,184,16\nMvaa 148,184,88\nMvaa 140,184,8\n"
"DL 252,52,70,0\nMvaa 132,184,80\nSA m0,s69,84776\nSA s70,s60,40960\nMvaa 124,184,0\nMvaa 84,188,4\n"
"Mvaa 92,188,84\nDL 252,52,71,0\nMvaa 100,188,12\nSA m0,s69,85808\nSA s71,s60,45056\nMvaa 108,188,92\n"
"Mvaa 116,188,20\ns_waitcnt lgkmcnt(0)\nMvaa 156,192,20\nDL 252,52,70,0\nMvaa 148,192,92\nSA m0,s69,86840\n"
"SA s70,s60,131072\nMvaa 140,192,12\nMvaa 132,192,84\nMvaa 124,192,4\nDL 252,52,71,0\nMvaa 84,196,40\n"
"SA s71,s60,135168\nMvaa 92,196,120\nMvaa 100,196,48\nMvaa 108,196,128\ns_barrier\nMvaa 116,196,56\n"
"SA m0,s69,1024\nTa 180,246,0\nMvaa 156,200,56\nTa 182,246,256\nMvaa 148,200,128\nDL 252,52,70,0\n"
"Mvaa 140,200,48\nSA m0,s69,2056\nTa 184,246,8\nSA s70,s60,139264\nMvaa 132,200,120\nTa 186,246,264\n"
"Mvaa 124,200,40\nTa 188,246,4128\nMvaa 84,204,44\nDL 252,52,71,0\nMvaa 92,204,124\nSA m0,s69,3088\n"
"Ta 190,246,4384\nSA s71,s60,143360\nMvaa 100,204,52\nTa 192,246,4136\nMvaa 108,204,132\nTa 194,246,4392\n"
"Mvaa 116,204,60\nDL 252,52,70,0\nMvaa 156,208,60\nSA m0,s69,4120\nTa 196,246,8256\nSA s70,s60,163840\n"
"Mvaa 148,208,132\nTa 198,246,8512\nMvaa 140,208,52\nTa 200,246,8264\nMvaa 132,208,124\nDL 252,52,71,0\n"
"Mvaa 124,208,44\ns_waitcnt lgkmcnt(6)\nSA m0,s69,5152\nTa 202,246,8520\nMvav 84,180,0\nSA s71,s60,167936\n"
"Ta 204,246,12384\nMvav 92,180,8\nTa 206,246,12640\nMvav 100,180,16\nDL 252,52,70,0\nMvav 108,180,24\n"
"SA m0,s69,6184\nSA s70,s60,172032\nTa 208,246,12392\nMvav 116,180,32\nTa 210,246,12648\nv_add_u32 v246, 0xffff3800, v246\n"
"Mvav 156,184,32\nTa 180,247,0\nMvav 148,184,24\nDL 252,52,71,0\nMvav 140,184,16\nSA m0,s69,7216\n"
"SA s71,s60,176128\nTa 182,247,256\nMvav 132,184,8\nSA s60,s78,5\ns_and_b32 s60, s60, 7\ns_lshl_b32 s60, s60, 7\n"
"Mvav 124,184,0\ns_waitcnt vmcnt(17)\nTa 184,247,8\nSA s60,s60,s63\nZava 212,242,160\nDL 252,52,70,0\n"
"Zava 232,240,160\ns_waitcnt vmcnt(17)\nSA m0,s69,8248\nTa 186,247,264\nZava 236,240,164\nSA s70,s60,0\n"
"Zava 216,242,164\ns_waitcnt vmcnt(16)\nZava 220,242,168\nDL 252,52,71,0\nZava 240,240,168\ns_waitcnt vmcnt(16)\n"
"SA s71,s60,4096\nZava 248,240,172\ns_waitcnt vmcnt(12)\nv_cmp_ne_u32 vcc, v255, v251\nZava 224,242,172\nZava 228,242,176\n"
"Zava 252,240,176\ns_and_b32 s73, vcc_lo, 2\ns_cbranch_scc1 STX1%=\nBKX1%=:\ns_waitcnt lgkmcnt(14)\nMvav 84,188,40\n"
"SA s81,s24,6\ns_and_b32 s81, s81, 7\ns_mul_i32 s81, s81, 20480\nMvav 92,188,48\nSA s81,s81,s97\nMvav 100,188,56\n"
"buffer_load_dwordx4 a[160:163], v253, s[56:59], s81 offen sc1\nMvav 108,188,64\nMvav 116,188,72\ns_waitcnt lgkmcnt(0)\nTa 188,247,4128\nMvav 156,192,72\n"
"Ta 190,247,4384\nMvav 148,192,64\nbuffer_load_dwordx4 a[164:167], v253, s[56:59], s81 offen offset:1024 sc1\nMvav 140,192,56\nMvav 132,192,48\nMvav 124,192,40\n"
"Ta 192,247,4136\nMvav 84,196,80\nbuffer_load_dwordx4 a[168:171], v253, s[56:59], s81 offen offset:2048 sc1\nMvav 92,196,88\nTa 194,247,4392\nMvav 100,196,96\n"
"Mvav 108,196,104\nMvav 116,196,112\nbuffer_load_dwordx4 a[172:175], v253, s[56:59], s81 offen offset:3072 sc1\nMvav 156,200,112\nSA s81,s81,4096\nTa 196,247,8256\n"
"Mvav 148,200,104\nTa 198,247,8512\nMvav 140,200,96\nMvav 132,200,88\nbuffer_load_dwordx4 a[176:179], v253, s[56:59], s81 offen sc1\nMvav 124,200,80\n"
"Ta 200,247,8264\nMvav 84,204,120\nTa 202,247,8520\nMvav 92,204,128\nMvav 100,204,136\nMvav 108,204,144\n"
"Mvav 116,204,152\nTa 204,247,12384\nMvav 156,208,152\nTa 206,247,12640\nMvav 148,208,144\nMvav 140,208,136\n"
"Mvav 132,208,128\nMvav 124,208,120\nTa 208,247,12392\ns_nop 0\nMvaa 84,180,96\ns_mov_b64 exec, 4\n"
"buffer_store_dword v254, v250, s[56:59], 0 offen sc1\ns_mov_b64 exec, -1\nMvaa 92,180,24\nTa 210,247,12648\nv_add_u32 v247, 0xffff3800, v247\nMvaa 100,180,104\n"
"Mvaa 108,180,32\nMvaa 116,180,112\nMvaa 156,184,112\nMvaa 148,184,32\nMvaa 140,184,104\nMvaa 132,184,24\n"
"Mvaa 124,184,96\ns_waitcnt lgkmcnt(1)\nMvaa 84,188,100\nMvaa 92,188,28\nMvaa 100,188,108\nMvaa 108,188,36\n"
"Mvaa 116,188,116\nMvaa 156,192,116\nMvaa 148,192,36\nMvaa 140,192,108\nMvaa 132,192,28\nMvaa 124,192,100\n"
"Mvaa 84,196,136\nMvaa 92,196,64\nMvaa 100,196,144\nMvaa 108,196,72\ns_waitcnt vmcnt(14) lgkmcnt(0)\ns_barrier\n"
"Mvaa 116,196,152\nSA m0,s69,130816\nTa 180,246,0\nMvaa 156,200,152\nTa 182,246,256\nMvaa 148,200,72\n"
"DL 252,52,70,0\nMvaa 140,200,144\nSA m0,s69,131848\nTa 184,246,8\nSA s70,s60,8192\nMvaa 132,200,64\n"
"Ta 186,246,264\nMvaa 124,200,136\nTa 188,246,4128\nMvaa 84,204,140\nDL 252,52,71,0\nMvaa 92,204,68\n"
"SA m0,s69,132880\nTa 190,246,4384\nSA s71,s60,12288\nMvaa 100,204,148\nTa 192,246,4136\nMvaa 108,204,76\n"
"CV 84,84,85\nCV 85,86,87\nCV 92,92,93\nCV 93,94,95\nMvaa 116,204,156\nDL 252,52,70,0\n"
"CV 100,100,101\nCV 101,102,103\nCV 108,108,109\nCV 109,110,111\nMvaa 156,208,156\nSA m0,s69,133912\n"
"Ta 194,246,4392\nCV 116,116,117\nCV 117,118,119\nMvaa 148,208,76\nSA s70,s60,32768\nTa 196,246,8256\n"
"Mvaa 140,208,148\nCV 118,156,157\nCV 119,158,159\nMvaa 132,208,68\nbuffer_store_dwordx4 v[116:119], v253, s[56:59], s72 offen sc1\nMvaa 124,208,140\n"
"s_waitcnt lgkmcnt(4)\nCV 110,148,149\nCV 111,150,151\nCV 102,140,141\nCV 103,142,143\nZvav 4,180,160\n"
"CV 94,132,133\nCV 95,134,135\nCV 86,124,125\nCV 87,126,127\nZvav 12,180,164\nTa 198,246,8512\n"
"Zvav 20,180,168\nbuffer_store_dwordx4 v[84:87], v253, s[56:59], s80 offen sc1\nZvav 28,180,172\nTa 200,246,8264\nZvav 36,180,176\nTa 202,246,8520\n"
"Zvav 76,184,176\nTa 204,246,12384\nZvav 68,184,172\nbuffer_store_dwordx4 v[92:95], v253, s[56:59], s80 offen offset:1024 sc1\nZvav 60,184,168\nTa 206,246,12640\n"
"Zvav 52,184,164\nTa 208,246,12392\nZvav 44,184,160\ns_waitcnt lgkmcnt(6)\nTa 210,246,12648\nv_add_u32 v246, 0xfffecd00, v246\n"
"Mvav 4,188,180\nbuffer_store_dwordx4 v[100:103], v253, s[56:59], s80 offen offset:2048 sc1\nMvav 12,188,184\nTa 180,247,0\nMvav 20,188,188\nTa 182,247,256\n"
"Mvav 28,188,192\nTa 184,247,8\nMvav 36,188,196\nbuffer_store_dwordx4 v[108:111], v253, s[56:59], s80 offen offset:3072 sc1\nMvav 76,192,196\nSA s80,s78,4\n"
"Ta 186,247,264\ns_and_b32 s80, s80, 7\nMvav 68,192,192\ns_mul_i32 s80, s80, 163840\nTa 188,247,4128\nSA s80,s80,s64\n"
"Mvav 60,192,188\nSA s72,s80,4096\nTa 190,247,4384\nMvav 52,192,184\nDL 252,52,71,0\nMvav 44,192,180\n"
"s_waitcnt lgkmcnt(6)\nSA m0,s69,134944\nSA s71,s60,36864\nMvav 4,196,200\nTa 192,247,4136\nMvav 12,196,204\n"
"Ta 194,247,4392\nMvav 20,196,208\nDL 252,52,70,0\nMvav 28,196,212\nSA m0,s69,135976\nSA s70,s60,40960\n"
"Mvav 36,196,216\nTa 196,247,8256\nMvav 76,200,216\nTa 198,247,8512\nMvav 68,200,212\nDL 252,52,71,0\n"
"Mvav 60,200,208\nSA m0,s69,137008\nSA s71,s60,45056\nMvav 52,200,204\nMvav 44,200,200\nTa 200,247,8264\n"
"Mvav 4,204,220\nDL 252,52,70,0\nMvav 12,204,224\nSA m0,s69,138040\nTa 202,247,8520\nSA s70,s60,131072\n"
"Mvav 20,204,228\nMvav 28,204,232\nMvav 36,204,236\nDL 252,52,71,0\nMvav 76,208,236\nSA s71,s60,135168\n"
"Ta 204,247,12384\nMvav 68,208,232\nTa 206,247,12640\nMvav 60,208,228\nMvav 52,208,224\nMvav 44,208,220\n"
"s_waitcnt lgkmcnt(3)\nTa 208,247,12392\nMvaa 4,180,0\nbuffer_load_dword v255, v248, s[56:59], 0 offen sc1\nMvaa 12,180,80\nTa 210,247,12648\n"
"v_add_u32 v247, 0xfffecd00, v247\nMvaa 20,180,8\nMvaa 28,180,88\nMvaa 36,180,16\nMvaa 76,184,16\nMvaa 68,184,88\n"
"Mvaa 60,184,8\nMvaa 52,184,80\nMvaa 44,184,0\nMvaa 4,188,4\nMvaa 12,188,84\nMvaa 20,188,12\n"
"Mvaa 28,188,92\nMvaa 36,188,20\nMvaa 76,192,20\nMvaa 68,192,92\nMvaa 60,192,12\nMvaa 52,192,84\n"
"Mvaa 44,192,4\nMvaa 4,196,40\nMvaa 12,196,120\nMvaa 20,196,48\nMvaa 28,196,128\ns_waitcnt vmcnt(20) lgkmcnt(0)\n"
"s_barrier\nMvaa 36,196,56\nSA m0,s69,64512\nTa 180,246,0\nMvaa 76,200,56\nTa 182,246,256\n"
"Mvaa 68,200,128\nDL 252,52,70,0\nMvaa 60,200,48\nSA m0,s69,65544\nTa 184,246,8\nSA s70,s60,139264\n"
"Mvaa 52,200,120\nTa 186,246,264\nMvaa 44,200,40\nTa 188,246,4128\nMvaa 4,204,44\nDL 252,52,71,0\n"
"Mvaa 12,204,124\nSA m0,s69,66576\nTa 190,246,4384\nSA s71,s60,143360\nMvaa 20,204,52\nTa 192,246,4136\n"
"Mvaa 28,204,132\nTa 194,246,4392\nMvaa 36,204,60\nDL 252,52,70,0\nMvaa 76,208,60\nSA m0,s69,67608\n"
"Ta 196,246,8256\nSA s70,s60,163840\nMvaa 68,208,132\nTa 198,246,8512\nMvaa 60,208,52\nTa 200,246,8264\n"
"Mvaa 52,208,124\nDL 252,52,71,0\nMvaa 44,208,44\ns_waitcnt lgkmcnt(6)\nSA m0,s69,68640\nTa 202,246,8520\n"
"Mvav 4,180,0\nSA s71,s60,167936\nTa 204,246,12384\nMvav 12,180,8\nTa 206,246,12640\nMvav 20,180,16\n"
"DL 252,52,70,0\nMvav 28,180,24\nSA m0,s69,69672\nSA s70,s60,172032\nTa 208,246,12392\nMvav 36,180,32\n"
"Ta 210,246,12648\nv_add_u32 v246, 0x1fb00, v246\nMvav 76,184,32\nTa 180,247,0\nMvav 68,184,24\nDL 252,52,71,0\n"
"Mvav 60,184,16\nSA m0,s69,70704\nSA s71,s60,176128\nTa 182,247,256\nMvav 52,184,8\nSA s60,s78,6\n"
"s_and_b32 s60, s60, 7\ns_lshl_b32 s60, s60, 7\nMvav 44,184,0\ns_waitcnt vmcnt(25)\nTa 184,247,8\nSA s60,s60,s63\n"
"Mava 212,242,160\nDL 252,52,70,0\nMava 232,240,160\ns_waitcnt vmcnt(25)\nSA m0,s69,71736\nTa 186,247,264\n"
"Mava 236,240,164\nSA s70,s60,0\nMava 216,242,164\ns_waitcnt vmcnt(24)\nMava 220,242,168\nDL 252,52,71,0\n"
"Mava 240,240,168\ns_waitcnt vmcnt(24)\nSA s71,s60,4096\nMava 248,240,172\ns_waitcnt vmcnt(8)\nv_cmp_ne_u32 vcc, v255, v251\n"
"Mava 224,242,172\nMava 228,242,176\nMava 252,240,176\ns_and_b32 s73, vcc_lo, 4\ns_cbranch_scc1 STX2%=\nBKX2%=:\n"
"s_waitcnt lgkmcnt(14)\nMvav 4,188,40\nSA s81,s24,5\ns_and_b32 s81, s81, 7\ns_mul_i32 s81, s81, 20480\nMvav 12,188,48\n"
"SA s81,s81,s97\nMvav 20,188,56\nbuffer_load_dwordx4 a[160:163], v253, s[56:59], s81 offen sc1\nMvav 28,188,64\nMvav 36,188,72\ns_waitcnt lgkmcnt(0)\n"
"Ta 188,247,4128\nMvav 76,192,72\nTa 190,247,4384\nMvav 68,192,64\nbuffer_load_dwordx4 a[164:167], v253, s[56:59], s81 offen offset:1024 sc1\nMvav 60,192,56\n"
"Mvav 52,192,48\nMvav 44,192,40\nTa 192,247,4136\nMvav 4,196,80\nbuffer_load_dwordx4 a[168:171], v253, s[56:59], s81 offen offset:2048 sc1\nMvav 12,196,88\n"
"Ta 194,247,4392\nMvav 20,196,96\nMvav 28,196,104\nMvav 36,196,112\nbuffer_load_dwordx4 a[172:175], v253, s[56:59], s81 offen offset:3072 sc1\nMvav 76,200,112\n"
"SA s81,s81,4096\nTa 196,247,8256\nMvav 68,200,104\nTa 198,247,8512\nMvav 60,200,96\nMvav 52,200,88\n"
"buffer_load_dwordx4 a[176:179], v253, s[56:59], s81 offen sc1\nMvav 44,200,80\nTa 200,247,8264\nMvav 4,204,120\nTa 202,247,8520\nMvav 12,204,128\n"
"Mvav 20,204,136\nMvav 28,204,144\nMvav 36,204,152\nTa 204,247,12384\nMvav 76,208,152\nTa 206,247,12640\n"
"Mvav 68,208,144\nMvav 60,208,136\nMvav 52,208,128\nMvav 44,208,120\nTa 208,247,12392\ns_nop 0\n"
"Mvaa 4,180,96\ns_mov_b64 exec, 8\nbuffer_store_dword v254, v250, s[56:59], 0 offen sc1\ns_mov_b64 exec, -1\nMvaa 12,180,24\nTa 210,247,12648\n"
"v_add_u32 v247, 0x1fb00, v247\nMvaa 20,180,104\nMvaa 28,180,32\nMvaa 36,180,112\nMvaa 76,184,112\nMvaa 68,184,32\n"
"Mvaa 60,184,104\nMvaa 52,184,24\nMvaa 44,184,96\ns_waitcnt lgkmcnt(1)\nMvaa 4,188,100\nMvaa 12,188,28\n"
"Mvaa 20,188,108\nMvaa 28,188,36\nMvaa 36,188,116\nMvaa 76,192,116\nMvaa 68,192,36\nMvaa 60,192,108\n"
"Mvaa 52,192,28\nMvaa 44,192,100\nMvaa 4,196,136\nMvaa 12,196,64\nMvaa 20,196,144\nMvaa 28,196,72\n"
"s_waitcnt lgkmcnt(0)\ns_barrier\nMvaa 36,196,152\nSA m0,s69,1024\nTa 180,246,0\nMvaa 76,200,152\n"
"Ta 182,246,256\nMvaa 68,200,72\nDL 252,52,70,0\nMvaa 60,200,144\nSA m0,s69,2056\nTa 184,246,8\n"
"SA s70,s60,8192\nMvaa 52,200,64\nTa 186,246,264\nMvaa 44,200,136\nTa 188,246,4128\nMvaa 4,204,140\n"
"DL 252,52,71,0\nMvaa 12,204,68\nSA m0,s69,3088\nTa 190,246,4384\nSA s71,s60,12288\nMvaa 20,204,148\n"
"Ta 192,246,4136\nMvaa 28,204,76\nCV 4,4,5\nCV 5,6,7\nCV 12,12,13\nCV 13,14,15\n"
"Mvaa 36,204,156\nDL 252,52,70,0\nCV 20,20,21\nCV 21,22,23\nCV 28,28,29\nCV 29,30,31\n"
"Mvaa 76,208,156\nSA m0,s69,4120\nTa 194,246,4392\nCV 36,36,37\nCV 37,38,39\nMvaa 68,208,76\n"
"SA s70,s60,32768\nTa 196,246,8256\nMvaa 60,208,148\nCV 38,76,77\nCV 39,78,79\nMvaa 52,208,68\n"
"buffer_store_dwordx4 v[36:39], v253, s[56:59], s72 offen sc1\nMvaa 44,208,140\ns_waitcnt lgkmcnt(4)\nCV 30,68,69\nCV 31,70,71\nCV 22,60,61\n"
"CV 23,62,63\nZvav 84,180,160\nCV 14,52,53\nCV 15,54,55\nCV 6,44,45\nCV 7,46,47\n"
"Zvav 92,180,164\nTa 198,246,8512\nZvav 100,180,168\nbuffer_store_dwordx4 v[4:7], v253, s[56:59], s80 offen sc1\nZvav 108,180,172\nTa 200,246,8264\n"
"Zvav 116,180,176\nTa 202,246,8520\nZvav 156,184,176\nTa 204,246,12384\nZvav 148,184,172\nbuffer_store_dwordx4 v[12:15], v253, s[56:59], s80 offen offset:1024 sc1\n"
"Zvav 140,184,168\nTa 206,246,12640\nZvav 132,184,164\nTa 208,246,12392\nZvav 124,184,160\ns_waitcnt lgkmcnt(6)\n"
"Ta 210,246,12648\nv_add_u32 v246, 0xfffefd00, v246\nMvav 84,188,180\nbuffer_store_dwordx4 v[20:23], v253, s[56:59], s80 offen offset:2048 sc1\nMvav 92,188,184\nTa 180,247,0\n"
"Mvav 100,188,188\nTa 182,247,256\nMvav 108,188,192\nTa 184,247,8\nMvav 116,188,196\nbuffer_store_dwordx4 v[28:31], v253, s[56:59], s80 offen offset:3072 sc1\n"
"Mvav 156,192,196\nSA s80,s78,5\nTa 186,247,264\ns_and_b32 s80, s80, 7\nMvav 148,192,192\ns_mul_i32 s80, s80, 163840\n"
"Ta 188,247,4128\nSA s80,s80,s64\nMvav 140,192,188\nSA s72,s80,4096\nTa 190,247,4384\nMvav 132,192,184\n"
"DL 252,52,71,0\nMvav 124,192,180\ns_waitcnt lgkmcnt(6)\nSA m0,s69,5152\nSA s71,s60,36864\nMvav 84,196,200\n"
"Ta 192,247,4136\nMvav 92,196,204\nTa 194,247,4392\nMvav 100,196,208\nDL 252,52,70,0\nMvav 108,196,212\n"
"SA m0,s69,6184\nSA s70,s60,40960\nMvav 116,196,216\nTa 196,247,8256\nMvav 156,200,216\nTa 198,247,8512\n"
"Mvav 148,200,212\nDL 252,52,71,0\nMvav 140,200,208\nSA m0,s69,7216\nSA s71,s60,45056\nMvav 132,200,204\n"
"Mvav 124,200,200\nTa 200,247,8264\nMvav 84,204,220\nDL 252,52,70,0\nMvav 92,204,224\nSA m0,s69,8248\n"
"Ta 202,247,8520\nSA s70,s60,131072\nMvav 100,204,228\nMvav 108,204,232\nMvav 116,204,236\nDL 252,52,71,0\n"
"Mvav 156,208,236\nSA s71,s60,135168\nTa 204,247,12384\nMvav 148,208,232\nTa 206,247,12640\nMvav 140,208,228\n"
"Mvav 132,208,224\nMvav 124,208,220\ns_waitcnt lgkmcnt(3)\nTa 208,247,12392\nMvaa 84,180,0\nbuffer_load_dword v255, v248, s[56:59], 0 offen sc1\n"
"Mvaa 92,180,80\nTa 210,247,12648\nv_add_u32 v247, 0xfffefd00, v247\nMvaa 100,180,8\nMvaa 108,180,88\nMvaa 116,180,16\n"
"Mvaa 156,184,16\nMvaa 148,184,88\nMvaa 140,184,8\nMvaa 132,184,80\nMvaa 124,184,0\nMvaa 84,188,4\n"
"Mvaa 92,188,84\nMvaa 100,188,12\nMvaa 108,188,92\nMvaa 116,188,20\nMvaa 156,192,20\nMvaa 148,192,92\n"
"Mvaa 140,192,12\nMvaa 132,192,84\nMvaa 124,192,4\nMvaa 84,196,40\nMvaa 92,196,120\nMvaa 100,196,48\n"
"Mvaa 108,196,128\ns_waitcnt vmcnt(20) lgkmcnt(0)\ns_barrier\nMvaa 116,196,56\nSA m0,s69,130816\nTa 180,246,0\n"
"Mvaa 156,200,56\nTa 182,246,256\nMvaa 148,200,128\nDL 252,52,70,0\nMvaa 140,200,48\nSA m0,s69,131848\n"
"Ta 184,246,8\nSA s70,s60,139264\nMvaa 132,200,120\nTa 186,246,264\nMvaa 124,200,40\nTa 188,246,4128\n"
"Mvaa 84,204,44\nDL 252,52,71,0\nMvaa 92,204,124\nSA m0,s69,132880\nTa 190,246,4384\nSA s71,s60,143360\n"
"Mvaa 100,204,52\nTa 192,246,4136\nMvaa 108,204,132\nTa 194,246,4392\nMvaa 116,204,60\nDL 252,52,70,0\n"
"Mvaa 156,208,60\nSA m0,s69,133912\nTa 196,246,8256\nSA s70,s60,163840\nMvaa 148,208,132\nTa 198,246,8512\n"
"Mvaa 140,208,52\nTa 200,246,8264\nMvaa 132,208,124\nDL 252,52,71,0\nMvaa 124,208,44\ns_waitcnt lgkmcnt(6)\n"
"SA m0,s69,134944\nTa 202,246,8520\nMvav 84,180,0\nSA s71,s60,167936\nTa 204,246,12384\nMvav 92,180,8\n"
"Ta 206,246,12640\nMvav 100,180,16\nDL 252,52,70,0\nMvav 108,180,24\nSA m0,s69,135976\nSA s70,s60,172032\n"
"Ta 208,246,12392\nMvav 116,180,32\nTa 210,246,12648\nv_add_u32 v246, 0xffff0800, v246\nMvav 156,184,32\nTa 180,247,0\n"
"Mvav 148,184,24\nDL 252,52,71,0\nMvav 140,184,16\nSA m0,s69,137008\nSA s71,s60,176128\nTa 182,247,256\n"
"Mvav 132,184,8\nSA s60,s78,7\ns_and_b32 s60, s60, 7\ns_lshl_b32 s60, s60, 7\nMvav 124,184,0\ns_waitcnt vmcnt(25)\n"
"Ta 184,247,8\nSA s60,s60,s63\nMava 212,242,160\nDL 252,52,70,0\nMava 232,240,160\ns_waitcnt vmcnt(25)\n"
"SA m0,s69,138040\nTa 186,247,264\nMava 236,240,164\nSA s70,s60,0\nMava 216,242,164\ns_waitcnt vmcnt(24)\n"
"Mava 220,242,168\nDL 252,52,71,0\nMava 240,240,168\ns_waitcnt vmcnt(24)\nSA s71,s60,4096\nMava 248,240,172\n"
"s_waitcnt vmcnt(8)\nv_cmp_ne_u32 vcc, v255, v251\nMava 224,242,172\nMava 228,242,176\nMava 252,240,176\ns_and_b32 s73, vcc_lo, 8\n"
"s_cbranch_scc1 STX3%=\nBKX3%=:\ns_waitcnt lgkmcnt(14)\nMvav 84,188,40\nSA s81,s24,4\ns_and_b32 s81, s81, 7\n"
"s_mul_i32 s81, s81, 20480\nMvav 92,188,48\nSA s81,s81,s97\nMvav 100,188,56\nbuffer_load_dwordx4 a[160:163], v253, s[56:59], s81 offen sc1\nMvav 108,188,64\n"
"Mvav 116,188,72\ns_waitcnt lgkmcnt(0)\nTa 188,247,4128\nMvav 156,192,72\nTa 190,247,4384\nMvav 148,192,64\n"
"buffer_load_dwordx4 a[164:167], v253, s[56:59], s81 offen offset:1024 sc1\nMvav 140,192,56\nMvav 132,192,48\nMvav 124,192,40\nTa 192,247,4136\nMvav 84,196,80\n"
"buffer_load_dwordx4 a[168:171], v253, s[56:59], s81 offen offset:2048 sc1\nMvav 92,196,88\nTa 194,247,4392\nMvav 100,196,96\nMvav 108,196,104\nMvav 116,196,112\n"
"buffer_load_dwordx4 a[172:175], v253, s[56:59], s81 offen offset:3072 sc1\nMvav 156,200,112\nSA s81,s81,4096\nTa 196,247,8256\nMvav 148,200,104\nTa 198,247,8512\n"
"Mvav 140,200,96\nMvav 132,200,88\nbuffer_load_dwordx4 a[176:179], v253, s[56:59], s81 offen sc1\nMvav 124,200,80\nTa 200,247,8264\nMvav 84,204,120\n"
"Ta 202,247,8520\nMvav 92,204,128\nMvav 100,204,136\nMvav 108,204,144\nMvav 116,204,152\nTa 204,247,12384\n"
"Mvav 156,208,152\nTa 206,247,12640\nMvav 148,208,144\nMvav 140,208,136\nMvav 132,208,128\nMvav 124,208,120\n"
"Ta 208,247,12392\ns_nop 0\nMvaa 84,180,96\ns_mov_b64 exec, 16\nbuffer_store_dword v254, v250, s[56:59], 0 offen sc1\ns_mov_b64 exec, -1\n"
"Mvaa 92,180,24\nTa 210,247,12648\nv_add_u32 v247, 0xffff0800, v247\nMvaa 100,180,104\nMvaa 108,180,32\nMvaa 116,180,112\n"
"Mvaa 156,184,112\nMvaa 148,184,32\nMvaa 140,184,104\nMvaa 132,184,24\nMvaa 124,184,96\ns_waitcnt lgkmcnt(1)\n"
"Mvaa 84,188,100\nMvaa 92,188,28\nMvaa 100,188,108\nMvaa 108,188,36\nMvaa 116,188,116\nMvaa 156,192,116\n"
"Mvaa 148,192,36\nMvaa 140,192,108\nMvaa 132,192,28\nMvaa 124,192,100\nMvaa 84,196,136\nMvaa 92,196,64\n"
"Mvaa 100,196,144\nMvaa 108,196,72\ns_waitcnt lgkmcnt(0)\ns_barrier\nMvaa 116,196,152\nSA m0,s69,97792\n"
"Ta 180,246,0\nMvaa 156,200,152\nTa 182,246,256\nMvaa 148,200,72\nDL 252,52,70,0\nMvaa 140,200,144\n"
"SA m0,s69,98824\nTa 184,246,8\nSA s70,s60,8192\nMvaa 132,200,64\nTa 186,246,264\nMvaa 124,200,136\n"
"Ta 188,246,4128\nMvaa 84,204,140\nDL 252,52,71,0\nMvaa 92,204,68\nSA m0,s69,99856\nTa 190,246,4384\n"
"SA s71,s60,12288\nMvaa 100,204,148\nTa 192,246,4136\nMvaa 108,204,76\nCV 84,84,85\nCV 85,86,87\n"
"CV 92,92,93\nCV 93,94,95\nMvaa 116,204,156\nDL 252,52,70,0\nCV 100,100,101\nCV 101,102,103\n"
"CV 108,108,109\nCV 109,110,111\nMvaa 156,208,156\nSA m0,s69,100888\nTa 194,246,4392\nCV 116,116,117\n"
"CV 117,118,119\nMvaa 148,208,76\nSA s70,s60,32768\nTa 196,246,8256\nMvaa 140,208,148\nCV 118,156,157\n"
"CV 119,158,159\nMvaa 132,208,68\nbuffer_store_dwordx4 v[116:119], v253, s[56:59], s72 offen sc1\nMvaa 124,208,140\ns_waitcnt lgkmcnt(4)\nCV 110,148,149\n"
"CV 111,150,151\nCV 102,140,141\nCV 103,142,143\nZvav 4,180,160\nCV 94,132,133\nCV 95,134,135\n"
"CV 86,124,125\nCV 87,126,127\nZvav 12,180,164\nTa 198,246,8512\nZvav 20,180,168\nbuffer_store_dwordx4 v[84:87], v253, s[56:59], s80 offen sc1\n"
"Zvav 28,180,172\nTa 200,246,8264\nZvav 36,180,176\nTa 202,246,8520\nZvav 76,184,176\nTa 204,246,12384\n"
"Zvav 68,184,172\nbuffer_store_dwordx4 v[92:95], v253, s[56:59], s80 offen offset:1024 sc1\nZvav 60,184,168\nTa 206,246,12640\nZvav 52,184,164\nTa 208,246,12392\n"
"Zvav 44,184,160\ns_waitcnt lgkmcnt(6)\nTa 210,246,12648\nv_add_u32 v246, 0x1fb00, v246\nMvav 4,188,180\nbuffer_store_dwordx4 v[100:103], v253, s[56:59], s80 offen offset:2048 sc1\n"
"Mvav 12,188,184\nTa 180,247,0\nMvav 20,188,188\nTa 182,247,256\nMvav 28,188,192\nTa 184,247,8\n"
"Mvav 36,188,196\nbuffer_store_dwordx4 v[108:111], v253, s[56:59], s80 offen offset:3072 sc1\nMvav 76,192,196\nSA s80,s78,6\nTa 186,247,264\ns_and_b32 s80, s80, 7\n"
"Mvav 68,192,192\ns_mul_i32 s80, s80, 163840\nTa 188,247,4128\nSA s80,s80,s64\nMvav 60,192,188\nSA s72,s80,4096\n"
"Ta 190,247,4384\nMvav 52,192,184\nDL 252,52,71,0\nMvav 44,192,180\ns_waitcnt lgkmcnt(6)\nSA m0,s69,101920\n"
"SA s71,s60,36864\nMvav 4,196,200\nTa 192,247,4136\nMvav 12,196,204\nTa 194,247,4392\nMvav 20,196,208\n"
"DL 252,52,70,0\nMvav 28,196,212\nSA m0,s69,102952\nSA s70,s60,40960\nMvav 36,196,216\nTa 196,247,8256\n"
"Mvav 76,200,216\nTa 198,247,8512\nMvav 68,200,212\nDL 252,52,71,0\nMvav 60,200,208\nSA m0,s69,103984\n"
"SA s71,s60,45056\nMvav 52,200,204\nMvav 44,200,200\nTa 200,247,8264\nMvav 4,204,220\nDL 252,52,70,0\n"
"Mvav 12,204,224\nSA m0,s69,105016\nTa 202,247,8520\nSA s70,s60,131072\nMvav 20,204,228\nMvav 28,204,232\n"
"Mvav 36,204,236\nDL 252,52,71,0\nMvav 76,208,236\nSA s71,s60,135168\nTa 204,247,12384\nMvav 68,208,232\n"
"Ta 206,247,12640\nMvav 60,208,228\nMvav 52,208,224\nMvav 44,208,220\ns_waitcnt lgkmcnt(3)\nTa 208,247,12392\n"
"Mvaa 4,180,0\nbuffer_load_dword v255, v248, s[56:59], 0 offen sc1\nMvaa 12,180,80\nTa 210,247,12648\nv_add_u32 v247, 0x1fb00, v247\nMvaa 20,180,8\n"
"Mvaa 28,180,88\nMvaa 36,180,16\nMvaa 76,184,16\nMvaa 68,184,88\nMvaa 60,184,8\nMvaa 52,184,80\n"
"Mvaa 44,184,0\nMvaa 4,188,4\nMvaa 12,188,84\nMvaa 20,188,12\nMvaa 28,188,92\nMvaa 36,188,20\n"
"Mvaa 76,192,20\nMvaa 68,192,92\nMvaa 60,192,12\nMvaa 52,192,84\nMvaa 44,192,4\nMvaa 4,196,40\n"
"Mvaa 12,196,120\nMvaa 20,196,48\nMvaa 28,196,128\ns_waitcnt vmcnt(20) lgkmcnt(0)\ns_barrier\nMvaa 36,196,56\n"
"SA m0,s69,1024\nTa 180,246,0\nMvaa 76,200,56\nTa 182,246,256\nMvaa 68,200,128\nDL 252,52,70,0\n"
"Mvaa 60,200,48\nSA m0,s69,2056\nTa 184,246,8\nSA s70,s60,139264\nMvaa 52,200,120\nTa 186,246,264\n"
"Mvaa 44,200,40\nTa 188,246,4128\nMvaa 4,204,44\nDL 252,52,71,0\nMvaa 12,204,124\nSA m0,s69,3088\n"
"Ta 190,246,4384\nSA s71,s60,143360\nMvaa 20,204,52\nTa 192,246,4136\nMvaa 28,204,132\nTa 194,246,4392\n"
"Mvaa 36,204,60\nDL 252,52,70,0\nMvaa 76,208,60\nSA m0,s69,4120\nTa 196,246,8256\nSA s70,s60,163840\n"
"Mvaa 68,208,132\nTa 198,246,8512\nMvaa 60,208,52\nTa 200,246,8264\nMvaa 52,208,124\nDL 252,52,71,0\n"
"Mvaa 44,208,44\ns_waitcnt lgkmcnt(6)\nSA m0,s69,5152\nTa 202,246,8520\nMvav 4,180,0\nSA s71,s60,167936\n"
"Ta 204,246,12384\nMvav 12,180,8\nTa 206,246,12640\nMvav 20,180,16\nDL 252,52,70,0\nMvav 28,180,24\n"
"SA m0,s69,6184\nSA s70,s60,172032\nTa 208,246,12392\nMvav 36,180,32\nTa 210,246,12648\nv_add_u32 v246, 0xffff7f00, v246\n"
"Mvav 76,184,32\nTa 180,247,0\nMvav 68,184,24\nDL 252,52,71,0\nMvav 60,184,16\nSA m0,s69,7216\n"
"SA s71,s60,176128\nTa 182,247,256\nMvav 52,184,8\nMvav 44,184,0\ns_waitcnt vmcnt(25)\nTa 184,247,8\n"
"Mava 212,242,160\nDL 252,52,70,0\nMava 232,240,160\ns_waitcnt vmcnt(25)\nSA m0,s69,8248\nTa 186,247,264\n"
"Mava 236,240,164\nMava 216,242,164\ns_waitcnt vmcnt(24)\nMava 220,242,168\nDL 252,52,71,0\nMava 240,240,168\n"
"s_waitcnt vmcnt(24)\nv_and_b32 v252, 0xf0, v253\nv_lshlrev_b32 v252, 7, v252\nMava 248,240,172\ns_waitcnt vmcnt(8)\nv_cmp_ne_u32 vcc, v255, v251\n"
"Mava 224,242,172\nMava 228,242,176\nMava 252,240,176\ns_and_b32 s73, vcc_lo, 16\ns_cbranch_scc1 STX4%=\nBKX4%=:\n"
"s_waitcnt lgkmcnt(14)\nMvav 4,188,40\nSA s81,s24,3\ns_and_b32 s81, s81, 7\ns_mul_i32 s81, s81, 20480\nMvav 12,188,48\n"
"SA s81,s81,s97\nMvav 20,188,56\nbuffer_load_dwordx4 a[160:163], v253, s[56:59], s81 offen sc1\nMvav 28,188,64\nMvav 36,188,72\ns_waitcnt lgkmcnt(0)\n"
"Ta 188,247,4128\nMvav 76,192,72\nTa 190,247,4384\nMvav 68,192,64\nbuffer_load_dwordx4 a[164:167], v253, s[56:59], s81 offen offset:1024 sc1\nMvav 60,192,56\n"
"Mvav 52,192,48\nMvav 44,192,40\nTa 192,247,4136\nMvav 4,196,80\nTa 194,247,4392\nMvav 12,196,88\n"
"buffer_load_dwordx4 a[168:171], v253, s[56:59], s81 offen offset:2048 sc1\nMvav 20,196,96\nMvav 28,196,104\nMvav 36,196,112\nTa 196,247,8256\nMvav 76,200,112\n"
"Ta 198,247,8512\nMvav 68,200,104\nbuffer_load_dwordx4 a[172:175], v253, s[56:59], s81 offen offset:3072 sc1\nMvav 60,200,96\nSA s81,s81,4096\nMvav 52,200,88\n"
"Mvav 44,200,80\nTa 200,247,8264\nMvav 4,204,120\nTa 202,247,8520\nMvav 12,204,128\nbuffer_load_dwordx4 a[176:179], v253, s[56:59], s81 offen sc1\n"
"Mvav 20,204,136\nMvav 28,204,144\nMvav 36,204,152\nTa 204,247,12384\nMvav 76,208,152\nTa 206,247,12640\n"
"Mvav 68,208,144\nMvav 60,208,136\nMvav 52,208,128\nMvav 44,208,120\nTa 208,247,12392\ns_nop 0\n"
"Mvaa 4,180,96\ns_mov_b64 exec, 32\nbuffer_store_dword v254, v250, s[56:59], 0 offen sc1\ns_mov_b64 exec, -1\nMvaa 12,180,24\nTa 210,247,12648\n"
"v_add_u32 v247, 0xffff7f00, v247\nMvaa 20,180,104\nMvaa 28,180,32\nMvaa 36,180,112\nMvaa 76,184,112\nMvaa 68,184,32\n"
"Mvaa 60,184,104\nMvaa 52,184,24\nMvaa 44,184,96\ns_waitcnt lgkmcnt(1)\nMvaa 4,188,100\nMvaa 12,188,28\n"
"Mvaa 20,188,108\nMvaa 28,188,36\nMvaa 36,188,116\nMvaa 76,192,116\nMvaa 68,192,36\nMvaa 60,192,108\n"
"Mvaa 52,192,28\nMvaa 44,192,100\nMvaa 4,196,136\nMvaa 12,196,64\nMvaa 20,196,144\nMvaa 28,196,72\n"
"s_waitcnt lgkmcnt(0)\ns_barrier\nMvaa 36,196,152\nTa 180,246,0\nMvaa 76,200,152\nTa 182,246,256\n"
"Mvaa 68,200,72\nTa 184,246,8\nMvaa 60,200,144\nTa 186,246,264\nMvaa 52,200,64\nTa 188,246,4128\n"
"Mvaa 44,200,136\nTa 190,246,4384\nMvaa 4,204,140\nTa 192,246,4136\nMvaa 12,204,68\nTa 194,246,4392\n"
"Mvaa 20,204,148\nTa 196,246,8256\nMvaa 28,204,76\nTa 198,246,8512\nMvaa 36,204,156\nCV 4,4,5\n"
"CV 5,6,7\nCV 12,12,13\nCV 13,14,15\nCV 20,20,21\nCV 21,22,23\nCV 28,28,29\n"
"CV 29,30,31\nCV 36,36,37\nCV 37,38,39\nMvaa 76,208,156\nTa 200,246,8264\nMvaa 68,208,76\n"
"Ta 202,246,8520\nMvaa 60,208,148\nTa 204,246,12384\nMvaa 52,208,68\nTa 206,246,12640\nMvaa 44,208,140\n"
"s_waitcnt lgkmcnt(8)\nCV 38,76,77\nCV 30,68,69\nCV 31,70,71\nCV 22,60,61\nCV 23,62,63\n"
"CV 14,52,53\nCV 15,54,55\nCV 6,44,45\nCV 7,46,47\nCV 39,78,79\nMaav 212,180,160\n"
"buffer_store_dwordx4 v[4:7], v253, s[56:59], s80 offen sc1\nMaav 216,180,164\nTa 208,246,12392\nMaav 220,180,168\nTa 210,246,12648\nv_add_u32 v246, 0xfffe8600, v246\n"
"Maav 224,180,172\nMaav 228,180,176\nbuffer_store_dwordx4 v[12:15], v253, s[56:59], s80 offen offset:1024 sc1\nMaav 252,184,176\nTa 180,247,0\nMaav 248,184,172\n"
"Ta 182,247,256\nMaav 240,184,168\nMaav 236,184,164\nbuffer_store_dwordx4 v[20:23], v253, s[56:59], s80 offen offset:2048 sc1\nMaav 232,184,160\nTa 184,247,8\n"
"Maav 212,188,180\nTa 186,247,264\nMaav 216,188,184\nMaav 220,188,188\nbuffer_store_dwordx4 v[28:31], v253, s[56:59], s80 offen offset:3072 sc1\nMaav 224,188,192\n"
"Maav 228,188,196\ns_waitcnt lgkmcnt(4)\nTa 188,247,4128\nMaav 252,192,196\nTa 190,247,4384\nMaav 248,192,192\n"
"buffer_store_dwordx4 v[36:39], v253, s[56:59], s72 offen sc1\nMaav 240,192,188\nSA s72,s98,0\nMaav 236,192,184\nMaav 232,192,180\nTa 192,247,4136\n"
"Maav 212,196,200\nTa 194,247,4392\nMaav 216,196,204\nMaav 220,196,208\nMaav 224,196,212\nMaav 228,196,216\n"
"Ta 196,247,8256\nMaav 252,200,216\nTa 198,247,8512\nMaav 248,200,212\nMaav 240,200,208\nMaav 236,200,204\n"
"Maav 232,200,200\nTa 200,247,8264\nMaav 212,204,220\nTa 202,247,8520\nMaav 216,204,224\nMaav 220,204,228\n"
"Maav 224,204,232\nMaav 228,204,236\nTa 204,247,12384\nMaav 252,208,236\nTa 206,247,12640\nMaav 248,208,232\n"
"Maav 240,208,228\nMaav 236,208,224\nMaav 232,208,220\ns_waitcnt lgkmcnt(3)\nTa 208,247,12392\nMaaa 212,180,0\n"
"buffer_load_dword v255, v248, s[56:59], 0 offen sc1\nMaaa 216,180,80\nTa 210,247,12648\nv_add_u32 v247, 0xfffe8600, v247\nMaaa 220,180,8\nMaaa 224,180,88\n"
"Maaa 228,180,16\nMaaa 252,184,16\nMaaa 248,184,88\nMaaa 240,184,8\nMaaa 236,184,80\nMaaa 232,184,0\n"
"Maaa 212,188,4\nMaaa 216,188,84\nMaaa 220,188,12\nMaaa 224,188,92\nMaaa 228,188,20\nMaaa 252,192,20\n"
"Maaa 248,192,92\nMaaa 240,192,12\nMaaa 236,192,84\nMaaa 232,192,4\nMaaa 212,196,40\nMaaa 216,196,120\n"
"Maaa 220,196,48\nMaaa 224,196,128\ns_waitcnt vmcnt(12) lgkmcnt(0)\ns_barrier\nMaaa 228,196,56\nTa 180,246,0\n"
"Maaa 252,200,56\nTa 182,246,256\nMaaa 248,200,128\nTa 184,246,8\nMaaa 240,200,48\nTa 186,246,264\n"
"Maaa 236,200,120\nTa 188,246,4128\nMaaa 232,200,40\ns_waitcnt vmcnt(1)\nTa 190,246,4384\ns_nop 0\n"
"Maaa 212,204,44\ns_mov_b64 exec, 64\nbuffer_store_dword v254, v250, s[56:59], 0 offen sc1\ns_mov_b64 exec, -1\nMaaa 216,204,124\nTa 192,246,4136\n"
"Maaa 220,204,52\nTa 194,246,4392\nMaaa 224,204,132\nTa 196,246,8256\nMaaa 228,204,60\nTa 198,246,8512\n"
"Maaa 252,208,60\nTa 200,246,8264\nMaaa 248,208,132\nTa 202,246,8520\nMaaa 240,208,52\nTa 204,246,12384\n"
"Maaa 236,208,124\nTa 206,246,12640\nMaaa 232,208,44\ns_waitcnt lgkmcnt(8)\nTa 208,246,12392\nMaav 212,180,0\n"
"Ta 210,246,12648\nv_lshrrev_b32 v246, 8, v253\nMaav 216,180,8\nv_lshl_add_u32 v252, v246, 4, v252\nMaav 220,180,16\nMaav 224,180,24\n"
"Maav 228,180,32\nTa 180,247,0\nMaav 252,184,32\nTa 182,247,256\nMaav 248,184,24\ns_waitcnt vmcnt(1)\n"
"v_cmp_ne_u32 vcc, v255, v251\nMaav 240,184,16\nMaav 236,184,8\nMaav 232,184,0\nTa 184,247,8\ns_and_b32 s73, vcc_lo, 32\n"
"s_cbranch_scc1 STX5%=\nBKX5%=:\nMaav 212,188,40\nSA s81,s24,2\ns_and_b32 s81, s81, 7\ns_mul_i32 s81, s81, 20480\n"
"Maav 216,188,48\nSA s81,s81,s97\nTa 186,247,264\nMaav 220,188,56\nbuffer_load_dwordx4 a[0:3], v253, s[56:59], s81 offen sc1\nMaav 224,188,64\n"
"Maav 228,188,72\ns_waitcnt lgkmcnt(2)\nTa 188,247,4128\nMaav 252,192,72\nTa 190,247,4384\nMaav 248,192,64\n"
"buffer_load_dwordx4 a[80:83], v253, s[56:59], s81 offen offset:1024 sc1\nMaav 240,192,56\nMaav 236,192,48\nMaav 232,192,40\nTa 192,247,4136\nMaav 212,196,80\n"
"buffer_load_dwordx4 a[8:11], v253, s[56:59], s81 offen offset:2048 sc1\nMaav 216,196,88\nTa 194,247,4392\nMaav 220,196,96\nMaav 224,196,104\nMaav 228,196,112\n"
"buffer_load_dwordx4 a[88:91], v253, s[56:59], s81 offen offset:3072 sc1\nMaav 252,200,112\nSA s81,s81,4096\nTa 196,247,8256\nMaav 248,200,104\nTa 198,247,8512\n"
"Maav 240,200,96\nMaav 236,200,88\nbuffer_load_dwordx4 a[16:19], v253, s[56:59], s81 offen sc1\nMaav 232,200,80\nTa 200,247,8264\nMaav 212,204,120\n"
"Ta 202,247,8520\nMaav 216,204,128\nMaav 220,204,136\nbuffer_load_dword v255, v248, s[56:59], 0 offen sc1\nMaav 224,204,144\nMaav 228,204,152\n"
"Ta 204,247,12384\nMaav 252,208,152\nTa 206,247,12640\nMaav 248,208,144\nMaav 240,208,136\nMaav 236,208,128\n"
"Maav 232,208,120\nTa 208,247,12392\nMaaa 212,180,96\nTa 210,247,12648\nMaaa 216,180,24\nMaaa 220,180,104\n"
"Maaa 224,180,32\nMaaa 228,180,112\ns_waitcnt lgkmcnt(3)\nMaaa 252,184,112\nMaaa 248,184,32\nMaaa 240,184,104\n"
"Maaa 236,184,24\nMaaa 232,184,96\nMaaa 212,188,100\nMaaa 216,188,28\nMaaa 220,188,108\nMaaa 224,188,36\n"
"Maaa 228,188,116\nMaaa 252,192,116\nMaaa 248,192,36\nMaaa 240,192,108\nMaaa 236,192,28\nMaaa 232,192,100\n"
"Maaa 212,196,136\nMaaa 216,196,64\nMaaa 220,196,144\nMaaa 224,196,72\nMaaa 228,196,152\nMaaa 252,200,152\n"
"Maaa 248,200,72\nMaaa 240,200,144\nMaaa 236,200,64\nMaaa 232,200,136\ns_waitcnt lgkmcnt(0)\nMaaa 212,204,140\n"
"Maaa 216,204,68\ns_waitcnt vmcnt(0)\nv_cmp_ne_u32 vcc, v255, v251\nMaaa 220,204,148\nMaaa 224,204,76\nMaaa 228,204,156\n"
"s_and_b32 s73, vcc_lo, 64\ns_cbranch_scc1 STX6%=\nBKX6%=:\nMaaa 252,208,156\nbuffer_load_dword v254, v250, s[56:59], 0 offen offset:64 sc1\nMaaa 248,208,76\n"
"SA s81,s24,1\ns_and_b32 s81, s81, 7\ns_mul_i32 s81, s81, 20480\nMaaa 240,208,148\nSA s81,s81,s97\nMaaa 236,208,68\n"
"Maaa 232,208,140\nMava 212,242,160\nbuffer_load_dwordx4 a[4:7], v253, s[56:59], s81 offen sc1\nMava 232,240,160\nMava 236,240,164\nMava 216,242,164\n"
"Mava 220,242,168\nbuffer_load_dwordx4 a[84:87], v253, s[56:59], s81 offen offset:1024 sc1\nMava 240,240,168\nMava 248,240,172\nMava 224,242,172\nMava 228,242,176\n"
"buffer_load_dwordx4 a[12:15], v253, s[56:59], s81 offen offset:2048 sc1\nMava 252,240,176\nMava 212,242,0\nMava 232,240,0\nMava 236,240,80\nbuffer_load_dwordx4 a[92:95], v253, s[56:59], s81 offen offset:3072 sc1\n"
"Mava 216,242,80\nSA s81,s81,4096\nMava 220,242,8\nMava 240,240,8\nMava 248,240,88\nbuffer_load_dwordx4 a[20:23], v253, s[56:59], s81 offen sc1\n"
"Mava 224,242,88\nMava 228,242,16\nMava 252,240,16\ns_waitcnt vmcnt(4)\nMava 212,242,4\nMava 232,240,4\n"
"s_waitcnt vmcnt(3)\nMava 236,240,84\nMava 216,242,84\ns_waitcnt vmcnt(2)\nMava 220,242,12\nMava 240,240,12\n"
"s_waitcnt vmcnt(1)\nAR 4,212\nAR 5,213\nMava 248,240,92\nAR 6,214\nAR 7,215\n"
"Mava 224,242,92\ns_waitcnt vmcnt(0)\nCV 4,4,5\nAR 44,232\nMava 228,242,20\nAR 45,233\n"
"AR 46,234\nMava 252,240,20\nAR 47,235\nCV 5,6,7\nbuffer_store_dword v254, v250, s[56:59], 0 offen sc1\nCV 6,44,45\n"
"CV 7,46,47\nbuffer_store_dwordx4 v[4:7], v252, s[36:39], s72 offen sc1\nAR 12,216\nAR 13,217\nAR 14,218\nAR 15,219\n"
"AR 52,236\nAR 53,237\nAR 54,238\nAR 55,239\nCV 12,12,13\nCV 13,14,15\n"
"CV 14,52,53\nCV 15,54,55\nSA s72,s98,32768\nbuffer_store_dwordx4 v[12:15], v252, s[36:39], s72 offen sc1\nAR 20,220\nAR 21,221\n"
"AR 22,222\nAR 23,223\nAR 60,240\nAR 61,241\nAR 62,242\nAR 63,243\n"
"CV 20,20,21\nCV 21,22,23\nCV 22,60,61\nCV 23,62,63\nSA s72,s98,65536\nbuffer_store_dwordx4 v[20:23], v252, s[36:39], s72 offen sc1\n"
"AR 28,224\nAR 29,225\nAR 30,226\nAR 31,227\nAR 68,248\nAR 69,249\n"
"AR 70,250\nAR 71,251\nCV 28,28,29\nCV 29,30,31\nCV 30,68,69\nCV 31,70,71\n"
"SA s72,s98,98304\nbuffer_store_dwordx4 v[28:31], v252, s[36:39], s72 offen sc1\nAR 36,228\nAR 37,229\nAR 38,230\nAR 39,231\n"
"AR 76,252\nAR 77,253\nAR 78,254\nAR 79,255\nCV 36,36,37\nCV 37,38,39\n"
"CV 38,76,77\nCV 39,78,79\nSA s72,s98,131072\nbuffer_store_dwordx4 v[36:39], v252, s[36:39], s72 offen sc1\ns_branch FE%=\nSTX0%=:\n"
"s_mov_b32 s73, 1\ns_mov_b32 s74, 3\ns_call_b64 s[94:95], SPINX%=\ns_branch BKX0%=\nSTX1%=:\ns_mov_b32 s73, 2\n"
"s_mov_b32 s74, 3\ns_call_b64 s[94:95], SPINX%=\ns_branch BKX1%=\nSTX2%=:\ns_mov_b32 s73, 4\ns_mov_b32 s74, 7\n"
"s_call_b64 s[94:95], SPINX%=\ns_branch BKX2%=\nSTX3%=:\ns_mov_b32 s73, 8\ns_mov_b32 s74, 15\ns_call_b64 s[94:95], SPINX%=\n"
"s_branch BKX3%=\nSTX4%=:\ns_mov_b32 s73, 16\ns_mov_b32 s74, 31\ns_call_b64 s[94:95], SPINX%=\ns_branch BKX4%=\n"
"STX5%=:\ns_mov_b32 s73, 32\ns_mov_b32 s74, 127\ns_call_b64 s[94:95], SPINX%=\ns_branch BKX5%=\nSTX6%=:\n"
"s_mov_b32 s73, 64\ns_mov_b32 s74, 127\ns_call_b64 s[94:95], SPINX%=\ns_branch BKX6%=\nSPINX%=:\ns_mov_b32 s75, 0\n"
"SLX%=:\nbuffer_load_dword v255, v248, s[56:59], 0 offen sc1\ns_waitcnt vmcnt(0)\nv_cmp_ne_u32 vcc, v255, v251\ns_nop 1\ns_and_b32 s92, vcc_lo, s73\n"
"s_cbranch_scc0 SRX%=\nSA s75,s75,1\ns_cmp_gt_u32 s75, 1048576\ns_cbranch_scc1 SRX%=\ns_sleep 1\ns_and_b32 s92, s75, 63\n"
"s_cbranch_scc1 SLX%=\nbuffer_load_dword v254, v250, s[56:59], 0 offen offset:64 sc1\ns_waitcnt vmcnt(0)\ns_mov_b32 exec_lo, s74\ns_mov_b32 exec_hi, 0\nbuffer_store_dword v254, v250, s[56:59], 0 offen sc1\n"
"s_mov_b64 exec, -1\ns_branch SLX%=\nSRX%=:\ns_setpc_b64 s[94:95]\nFE%=:\ns_waitcnt vmcnt(0) lgkmcnt(0)\n"
"s_mov_b32 %[f0], s88\nEND%=:\n.purgem SA\n.purgem DL\n.purgem Ra\n.purgem Ta\n"
".purgem Zvaa\n.purgem AR\n.purgem MU\n.purgem CV\n.purgem Mvaa\n.purgem DN\n"
".purgem Zvav\n.purgem Mvav\n.purgem Cvav\n.purgem EX\n.purgem Zaav\n.purgem Maav\n"
".purgem Zava\n.purgem Mava\n.purgem Maaa\n"
    : [f0] "=&s"(f0), [ka] "+s"(ka), [bid] "+s"(bid), [wid] "+s"(wid)
    : [lds] "s"((unsigned)(size_t)lds)
    : "v0", "v1", "v2", "v3", "v4", "v5", "v6", "v7", "v8", "v9", "v10", "v11", "v12", "v13", "v14", "v15", "v16", "v17", "v18", "v19", "v20", "v21", "v22", "v23", "v24", "v25", "v26", "v27", "v28", "v29", "v30", "v31", "v32", "v33", "v34", "v35", "v36", "v37", "v38", "v39", "v40", "v41", "v42", "v43", "v44", "v45", "v46", "v47", "v48", "v49", "v50", "v51", "v52", "v53", "v54", "v55", "v56", "v57", "v58", "v59", "v60", "v61", "v62", "v63", "v64", "v65", "v66", "v67", "v68", "v69", "v70", "v71", "v72", "v73", "v74", "v75", "v76", "v77", "v78", "v79", "v80", "v81", "v82", "v83", "v84", "v85", "v86", "v87", "v88", "v89", "v90", "v91", "v92", "v93", "v94", "v95", "v96", "v97", "v98", "v99", "v100", "v101", "v102", "v103", "v104", "v105", "v106", "v107", "v108", "v109", "v110", "v111", "v112", "v113", "v114", "v115", "v116", "v117", "v118", "v119", "v120", "v121", "v122", "v123", "v124", "v125", "v126", "v127", "v128", "v129", "v130", "v131", "v132", "v133", "v134", "v135", "v136", "v137", "v138", "v139", "v140", "v141", "v142", "v143", "v144", "v145", "v146", "v147", "v148", "v149", "v150", "v151", "v152", "v153", "v154", "v155", "v156", "v157", "v158", "v159", "v160", "v161", "v162", "v163", "v164", "v165", "v166", "v167", "v168", "v169", "v170", "v171", "v172", "v173", "v174", "v175", "v176", "v177", "v178", "v179", "v180", "v181", "v182", "v183", "v184", "v185", "v186", "v187", "v188", "v189", "v190", "v191", "v192", "v193", "v194", "v195", "v196", "v197", "v198", "v199", "v200", "v201", "v202", "v203", "v204", "v205", "v206", "v207", "v208", "v209", "v210", "v211", "v212", "v213", "v214", "v215", "v216", "v217", "v218", "v219", "v220", "v221", "v222", "v223", "v224", "v225", "v226", "v227", "v228", "v229", "v230", "v231", "v232", "v233", "v234", "v235", "v236", "v237", "v238", "v239", "v240", "v241", "v242", "v243", "v244", "v245", "v246", "v247", "v248", "v249", "v250", "v251", "v252", "v253", "v254", "v255", "a0", "a1", "a2", "a3", "a4", "a5", "a6", "a7", "a8", "a9", "a10", "a11", "a12", "a13", "a14", "a15", "a16", "a17", "a18", "a19", "a20", "a21", "a22", "a23", "a24", "a25", "a26", "a27", "a28", "a29", "a30", "a31", "a32", "a33", "a34", "a35", "a36", "a37", "a38", "a39", "a40", "a41", "a42", "a43", "a44", "a45", "a46", "a47", "a48", "a49", "a50", "a51", "a52", "a53", "a54", "a55", "a56", "a57", "a58", "a59", "a60", "a61", "a62", "a63", "a64", "a65", "a66", "a67", "a68", "a69", "a70", "a71", "a72", "a73", "a74", "a75", "a76", "a77", "a78", "a79", "a80", "a81", "a82", "a83", "a84", "a85", "a86", "a87", "a88", "a89", "a90", "a91", "a92", "a93", "a94", "a95", "a96", "a97", "a98", "a99", "a100", "a101", "a102", "a103", "a104", "a105", "a106", "a107", "a108", "a109", "a110", "a111", "a112", "a113", "a114", "a115", "a116", "a117", "a118", "a119", "a120", "a121", "a122", "a123", "a124", "a125", "a126", "a127", "a128", "a129", "a130", "a131", "a132", "a133", "a134", "a135", "a136", "a137", "a138", "a139", "a140", "a141", "a142", "a143", "a144", "a145", "a146", "a147", "a148", "a149", "a150", "a151", "a152", "a153", "a154", "a155", "a156", "a157", "a158", "a159", "a160", "a161", "a162", "a163", "a164", "a165", "a166", "a167", "a168", "a169", "a170", "a171", "a172", "a173", "a174", "a175", "a176", "a177", "a178", "a179", "a180", "a181", "a182", "a183", "a184", "a185", "a186", "a187", "a188", "a189", "a190", "a191", "a192", "a193", "a194", "a195", "a196", "a197", "a198", "a199", "a200", "a201", "a202", "a203", "a204", "a205", "a206", "a207", "a208", "a209", "a210", "a211", "a212", "a213", "a214", "a215", "a216", "a217", "a218", "a219", "a220", "a221", "a222", "a223", "a224", "a225", "a226", "a227", "a228", "a229", "a230", "a231", "a232", "a233", "a234", "a235", "a236", "a237", "a238", "a239", "a240", "a241", "a242", "a243", "a244", "a245", "a246", "a247", "a248", "a249", "a250", "a251", "a252", "a253", "a254", "a255", "s8", "s9", "s10", "s11", "s12", "s13", "s14", "s15", "s16", "s17", "s18", "s19", "s20", "s21", "s22", "s23", "s24", "s25", "s26", "s27", "s28", "s29", "s30", "s31", "s33", "s34", "s35", "s36", "s37", "s38", "s39", "s40", "s41", "s42", "s43", "s44", "s45", "s46", "s47", "s48", "s49", "s50", "s51", "s52", "s53", "s54", "s55", "s56", "s57", "s58", "s59", "s60", "s61", "s62", "s63", "s64", "s65", "s66", "s67", "s68", "s69", "s70", "s71", "s72", "s73", "s74", "s75", "s76", "s77", "s78", "s79", "s80", "s81", "s82", "s83", "s84", "s85", "s86", "s87", "s88", "s89", "s90", "s91", "s92", "s93", "s94", "s95", "s96", "s97", "s98", "s99", "vcc", "scc", "memory");
  if (f0) {
    const int bl = bid >> 5, pair = (bid & 1) * 4 + ((bid >> 3) & 3), own = (pair + bl) & 7, br = (bid >> 2) & 1, b = ((bid >> 1) & 1) * 8 + bl;
    u16* part = ka[10] + (size_t)(br * 16 + b) * 671744;
    const int lane = __builtin_amdgcn_mbcnt_hi(~0u, __builtin_amdgcn_mbcnt_lo(~0u, 0u));
    __syncthreads();
    slow_path(ka[5 * br] + (size_t)b * 81920, ka[5 * br + 3], ka[5 * br + 1] + (size_t)b * 2097152, ka[5 * br + 2] + (size_t)b * 2097152, ka[5 * br + 4],
              part, pair, wid, (float*)(lds + 1024 + wid * 8192), lane);
    asm volatile("s_waitcnt vmcnt(0)" ::: "memory");
    __syncthreads();
    slow_reduce(part, ka[11] + (size_t)b * 163840 + br * 512 + own * 64, pair, own, wid, lane);
  }
}

extern "C" void kernel_launch(void* const* d_in, const int* in_sizes, int n_in, void* d_out, int out_size, void* d_ws, size_t ws_size, hipStream_t stream) {
  (void)in_sizes; (void)n_in; (void)out_size; (void)ws_size;
  (void)hipFuncSetAttribute((const void*)fused_kernel, hipFuncAttributeMaxDynamicSharedMemorySize, 163840);
  fused_kernel<<<256, 256, 163840, stream>>>((const u16*)d_in[0], (const u16*)d_in[1], (const u16*)d_in[2], (const u16*)d_in[3], (const u16*)d_in[4],
                                             (const u16*)d_in[5], (const u16*)d_in[6], (const u16*)d_in[7], (const u16*)d_in[8], (const u16*)d_in[9], (u16*)d_ws, (u16*)d_out);
}
```
